```python
import torch

def _softmax(x, axis=-1):
    return torch.softmax(x, dim=axis)
def _gelu(x):
    return torch.nn.functional.gelu(x, approximate="tanh")
def _silu(x):
    return torch.nn.functional.silu(x)
def _sigmoid(x):
    return torch.sigmoid(x)
def _layernorm(x, axis=-1, eps=1e-5):
    m = x.mean(dim=axis, keepdim=True)
    v = x.var(dim=axis, keepdim=True, unbiased=False)
    return (x - m) / torch.sqrt(v + eps)
def _scatter_add(tgt, idx, src, axis=0):
    return tgt.clone().index_add_(axis, idx, src)
def _segment_sum(src, seg, n):
    out = torch.zeros((n,) + src.shape[1:], dtype=src.dtype)
    return out.index_add_(0, seg, src)
def _pad(x, pw):
    flat = tuple(v for pair in reversed(pw) for v in pair)
    return torch.nn.functional.pad(x, flat)

kernel_name = 'recurrent_scan__sliding_block_cached__glu_gated_bilinear__decode'

def setup_inputs():

    _w = (lambda a: (lambda t: t.to(torch.bfloat16) if t.is_floating_point() else t)(torch.as_tensor(a)))
    def _A_setup():
        B, Sq, D = 8, 1, 512
        Hq, Hkv, Sk, Dh = 32, 8, 4096, 128
        assert Hq % Hkv == 0, (Hq, Hkv)
        return {
            "x": _w((lambda *s: torch.randn(*s, dtype=torch.bfloat16) * 0.05)(B, Sq, D)),
            "k_cache": _w((lambda *s: torch.randn(*s, dtype=torch.bfloat16) * 3.2)(B, Hkv, Sk, Dh)),
            "v_cache": _w((lambda *s: torch.randn(*s, dtype=torch.bfloat16) * 3.2)(B, Hkv, Sk, Dh)),
            "wq": _w((lambda *s: torch.randn(*s, dtype=torch.bfloat16) * 3.2)(D, Hq * Dh) * (D ** -0.5)),
            "wo": _w((lambda *s: torch.randn(*s, dtype=torch.bfloat16) * 3.2)(Hq * Dh, D) * ((Hq * Dh) ** -0.5)),
        }
    _A_in = _A_setup()

    def _B_setup():
        B, S, D, F = 8, 1, 512, 1792
        return {
            "x": _w((lambda *s: torch.randn(*s, dtype=torch.bfloat16) * 0.05)(B, S, D)),
            "w_gate": _w((lambda *s: torch.randn(*s, dtype=torch.bfloat16) * 0.4)(D, F) * (D ** -0.5)),
            "w_up": _w((lambda *s: torch.randn(*s, dtype=torch.bfloat16) * 0.4)(D, F) * (D ** -0.5)),
            "w_down": _w((lambda *s: torch.randn(*s, dtype=torch.bfloat16) * 0.4)(F, D) * (F ** -0.5)),
        }
    _B_in = _B_setup()

    return {
        "A_x": _A_in["x"],
        "A_k_cache": _A_in["k_cache"],
        "A_v_cache": _A_in["v_cache"],
        "A_wq": _A_in["wq"],
        "A_wo": _A_in["wo"],
        "B_x": _B_in["x"],
        "B_w_gate": _B_in["w_gate"],
        "B_w_up": _B_in["w_up"],
        "B_w_down": _B_in["w_down"],
    }

def reference(A_x, A_k_cache, A_v_cache, A_wq, A_wo, B_x, B_w_gate, B_w_up, B_w_down):
    def _A_ref(x, k_cache, v_cache, wq, wo):
        B, Sq, D = x.shape
        Hq, Dh = 32, 128
        Hkv, Sk = k_cache.shape[1], k_cache.shape[2]
        W = min(4096, max(256, Sk // 4))
        rep = Hq // Hkv
        q = (lambda x, *d: x.permute(*d))((x @ wq).reshape(B, Sq, Hq, Dh), 0, 2, 1, 3)
        k = (lambda x, n, axis: torch.repeat_interleave(x, n, dim=axis))(k_cache[:, :, Sk - W:, :], rep, axis=1)
        v = (lambda x, n, axis: torch.repeat_interleave(x, n, dim=axis))(v_cache[:, :, Sk - W:, :], rep, axis=1)
        scores = torch.einsum("bhqd,bhkd->bhqk", q, k) * (Dh ** -0.5)
        p = _softmax(scores, axis=-1)
        o = torch.einsum("bhqk,bhkd->bhqd", p, v)
        return (lambda x, *d: x.permute(*d))(o, 0, 2, 1, 3).reshape(B, Sq, Hq * Dh) @ wo

    def _B_ref(x, w_gate, w_up, w_down):
        g = torch.einsum("bsd,df->bsf", x, w_gate)
        u = torch.einsum("bsd,df->bsf", x, w_up)
        h = g * u
        return torch.einsum("bsf,fd->bsd", h, w_down)

    x = A_x
    xn = _layernorm(x, axis=-1)
    s = _A_ref(xn, A_k_cache, A_v_cache, A_wq, A_wo)
    g = _silu(_B_ref(xn, B_w_gate, B_w_up, B_w_down))
    return x + s * g
```

```cpp
#include <hip/hip_runtime.h>
#include <hip/hip_bf16.h>
#include <cstdint>
#define MIMIC 1
typedef unsigned short u16;
__device__ __forceinline__ float bf2f(u16 v) { return __uint_as_float((unsigned)v << 16); }
__device__ __forceinline__ u16 f2bf(float f) { __hip_bfloat16 h = __float2bfloat16(f); return *reinterpret_cast<u16*>(&h); }
__device__ __forceinline__ float rbf(float f) {
  return bf2f(f2bf(f));
}
constexpr int B = 8, D = 512, HQ = 32, HKV = 8, SK = 4096, DH = 128, W = 1024, F = 1792;

__global__ void k_ln(const u16* x, float* xn) {
  int b = blockIdx.x, t = threadIdx.x;
  __shared__ float red[512];
  float v = bf2f(x[b * D + t]);
  red[t] = v; __syncthreads();
  for (int s = 256; s > 0; s >>= 1) { if (t < s) red[t] += red[t + s]; __syncthreads(); }
  float mean = red[0] / D; __syncthreads();
  float dv = v - mean;
  red[t] = dv * dv; __syncthreads();
  for (int s = 256; s > 0; s >>= 1) { if (t < s) red[t] += red[t + s]; __syncthreads(); }
  float var = red[0] / D;
  float m = rbf(mean), vr = rbf(var);
  float num = rbf(v - m);
  float den = rbf(sqrtf(rbf(vr + 1e-5f)));
  xn[b * D + t] = rbf(num / den);
}
__global__ void k_gemv(const float* a, const u16* w, float* out, int K, int N) {
  int n = blockIdx.x * blockDim.x + threadIdx.x;
  if (n >= N) return;
  float acc[B];
  for (int b = 0; b < B; ++b) acc[b] = 0.f;
  for (int k = 0; k < K; ++k) {
    float wv = bf2f(w[(size_t)k * N + n]);
    for (int b = 0; b < B; ++b) acc[b] = fmaf(a[b * K + k], wv, acc[b]);
  }
  for (int b = 0; b < B; ++b) out[b * N + n] = rbf(acc[b]);
}
__global__ void k_mul(const float* g, const float* u, float* h, int n) {
  int i = blockIdx.x * blockDim.x + threadIdx.x;
  if (i < n) h[i] = rbf(g[i] * u[i]);
}
__global__ void k_attn(const float* q, const u16* kc, const u16* vc, float* o) {
  int h = blockIdx.x, b = blockIdx.y, t = threadIdx.x;
  int kvh = h / (HQ / HKV);
  __shared__ float sc[W];
  __shared__ float red[W];
  __shared__ float qs[DH];
  if (t < DH) qs[t] = q[b * HQ * DH + h * DH + t];
  __syncthreads();
  const u16* kp = kc + ((size_t)(b * HKV + kvh) * SK + (SK - W) + t) * DH;
  float s = 0.f;
  for (int d = 0; d < DH; ++d) s = fmaf(qs[d], bf2f(kp[d]), s);
  s = rbf(rbf(s) * 0.08838834764831845f);
  red[t] = s; __syncthreads();
  for (int st = 512; st > 0; st >>= 1) { if (t < st) red[t] = fmaxf(red[t], red[t + st]); __syncthreads(); }
  float m = red[0]; __syncthreads();
  float e = __expf(s - m);
  red[t] = e; __syncthreads();
  for (int st = 512; st > 0; st >>= 1) { if (t < st) red[t] += red[t + st]; __syncthreads(); }
  float l = red[0];
  sc[t] = rbf(e / l);
  __syncthreads();
  if (t < DH) {
    const u16* vp = vc + ((size_t)(b * HKV + kvh) * SK + (SK - W)) * DH + t;
    float acc = 0.f;
    for (int j = 0; j < W; ++j) acc = fmaf(sc[j], bf2f(vp[(size_t)j * DH]), acc);
    o[b * HQ * DH + h * DH + t] = rbf(acc);
  }
}
__global__ void k_final(const u16* x, const float* s, const float* d, u16* out) {
  int i = blockIdx.x * blockDim.x + threadIdx.x;
  if (i >= B * D) return;
  float dv = d[i];
  float g = rbf(dv / (1.f + __expf(-dv)));
  float t = rbf(s[i] * g);
  out[i] = f2bf(bf2f(x[i]) + t);
}
extern "C" void kernel_launch(void* const* d_in, const int* in_sizes, int n_in, void* d_out, int out_size, void* d_ws, size_t ws_size, hipStream_t stream) {
  const u16* x = (const u16*)d_in[0];
  const u16* kc = (const u16*)d_in[1];
  const u16* vc = (const u16*)d_in[2];
  const u16* wq = (const u16*)d_in[3];
  const u16* wo = (const u16*)d_in[4];
  const u16* wg = (const u16*)d_in[6];
  const u16* wu = (const u16*)d_in[7];
  const u16* wd = (const u16*)d_in[8];
  float* ws = (float*)d_ws;
  float* xn = ws; float* q = xn + B * D; float* g = q + B * HQ * DH; float* u = g + B * F; float* h = u + B * F;
  float* o = h + B * F; float* s = o + B * HQ * DH; float* dd = s + B * D;
  k_ln<<<B, 512, 0, stream>>>(x, xn);
  k_gemv<<<HQ * DH / 64, 64, 0, stream>>>(xn, wq, q, D, HQ * DH);
  k_gemv<<<F / 64, 64, 0, stream>>>(xn, wg, g, D, F);
  k_gemv<<<F / 64, 64, 0, stream>>>(xn, wu, u, D, F);
  k_mul<<<(B * F + 255) / 256, 256, 0, stream>>>(g, u, h, B * F);
  k_attn<<<dim3(HQ, B), W, 0, stream>>>(q, kc, vc, o);
  k_gemv<<<D / 64, 64, 0, stream>>>(o, wo, s, HQ * DH, D);
  k_gemv<<<D / 64, 64, 0, stream>>>(h, wd, dd, F, D);
  k_final<<<(B * D + 255) / 256, 256, 0, stream>>>(x, s, dd, (u16*)d_out);
}
```

```cpp
#include <hip/hip_runtime.h>
#include <hip/hip_bf16.h>
#include <stdint.h>

#define F1_DELAY 16
#define STAMPS 0

typedef unsigned short u16;
typedef unsigned long long u64;
typedef unsigned v4u __attribute__((ext_vector_type(4)));
typedef unsigned v2u __attribute__((ext_vector_type(2)));
typedef float v4f __attribute__((ext_vector_type(4)));
typedef __bf16 bf16x8 __attribute__((ext_vector_type(8)));
typedef __bf16 bf16x2 __attribute__((ext_vector_type(2)));
typedef float v2f __attribute__((ext_vector_type(2)));

constexpr int NB = 8, DM = 512, HQ = 32, HKV = 8, SK = 4096, DH = 128, WIN = 1024, FF = 1792;
constexpr int NWG = 256, NWAVE = 11, NTHR = NWAVE * 64;
constexpr int W_C = 0, W_Q = 1, W_F = 2, W_A0 = 3;

constexpr unsigned WS_EPOCH = 0;
constexpr unsigned WS_FLAG1 = 256;
constexpr unsigned WS_FLAG2 = WS_FLAG1 + 2048;
constexpr unsigned WS_LSE = 8192;
constexpr unsigned WS_QP = 16384;
constexpr unsigned WS_GU = WS_QP + 8 * 8 * 4 * 8 * 256;
constexpr unsigned WS_OP = WS_GU + 2 * 4 * 8 * 28 * 256;
constexpr unsigned WS_SP = WS_OP + 8 * 4 * 8 * 4 * 2 * 256;
constexpr unsigned WS_DP = WS_SP + 256 * 64 * 64;
constexpr unsigned WS_C2 = WS_DP + 256 * 32 * 64;
constexpr unsigned WS_C3 = WS_C2 + 512 * 128;
constexpr unsigned WS_END = WS_C3 + 768 * 128;
constexpr unsigned WS_STAMP = 8u << 20;

constexpr unsigned SENT = 0xFFFFFFFFu;
constexpr u64 EPOCH_STEP = 0x9E3779B97F4A7C15ull;
constexpr unsigned SPIN_CAP = 1u << 18;
constexpr int AUX_SC1 = 16;

constexpr int L_W = 0;
constexpr int L_Q = 64;
constexpr int L_MX = L_Q + 1024;
constexpr int L_LS = L_MX + 128;
constexpr int L_O = L_LS + 128;
constexpr int L_END = L_O + 8 * 512 * 4;
constexpr int LDS_BYTES = 96 * 1024;
static_assert(L_O % 16 == 0 && L_END <= LDS_BYTES, "lds");
constexpr int SW_CLEARED = 0, SW_FLAGOK = 1, SW_QREADY = 2, SW_ARRIVE = 3, SW_ATTDONE = 4;
constexpr int L_T = 32;

__device__ __forceinline__ float bflo(unsigned w) { return __uint_as_float(w << 16); }
__device__ __forceinline__ float bfhi(unsigned w) { return __uint_as_float(w & 0xffff0000u); }
__device__ __forceinline__ unsigned pk2(float lo, float hi) {
  v2f f = {lo, hi};
  bf16x2 b = __builtin_convertvector(f, bf16x2);
  return __builtin_bit_cast(unsigned, b);
}
__device__ __forceinline__ float rbf(float x) { return __uint_as_float(pk2(x, 0.f) << 16); }
__device__ __forceinline__ v4f mfma(v4u a, v4u b, v4f c) {
  return __builtin_amdgcn_mfma_f32_16x16x32_bf16(__builtin_bit_cast(bf16x8, a), __builtin_bit_cast(bf16x8, b), c, 0, 0, 0);
}
template <int CTRL> __device__ __forceinline__ float dpp(float x) {
  return __builtin_bit_cast(float, __builtin_amdgcn_mov_dpp(__builtin_bit_cast(int, x), CTRL, 0xf, 0xf, true));
}
constexpr int XOR1 = 0xB1, XOR2 = 0x4E, HMIR = 0x141, ROR8 = 0x128, ROR4 = 0x124;
__device__ __forceinline__ float xrow_max(float x) {
  auto s = __builtin_amdgcn_permlane16_swap(__float_as_uint(x), __float_as_uint(x), false, false);
  x = fmaxf(__uint_as_float(s[0]), __uint_as_float(s[1]));
  auto t = __builtin_amdgcn_permlane32_swap(__float_as_uint(x), __float_as_uint(x), false, false);
  return fmaxf(__uint_as_float(t[0]), __uint_as_float(t[1]));
}
__device__ __forceinline__ float xrow_sum(float x) {
  auto s = __builtin_amdgcn_permlane16_swap(__float_as_uint(x), __float_as_uint(x), false, false);
  x = __uint_as_float(s[0]) + __uint_as_float(s[1]);
  auto t = __builtin_amdgcn_permlane32_swap(__float_as_uint(x), __float_as_uint(x), false, false);
  return __uint_as_float(t[0]) + __uint_as_float(t[1]);
}
__device__ __forceinline__ float wave_sum(float x) {
  x += dpp<XOR1>(x); x += dpp<XOR2>(x); x += dpp<HMIR>(x); x += dpp<ROR8>(x);
  return xrow_sum(x);
}
__device__ __forceinline__ v4u ldnt(const void* p) { return __builtin_nontemporal_load((const v4u*)p); }
#define BARRIER() do { asm volatile("s_waitcnt lgkmcnt(0)" ::: "memory"); __builtin_amdgcn_s_barrier(); asm volatile("" ::: "memory"); } while (0)
#define SCHED_FENCE() __builtin_amdgcn_sched_barrier(0)

typedef __amdgpu_buffer_rsrc_t rsrc_t;
__device__ __forceinline__ v4u mb_ld(rsrc_t rs, unsigned off) { return __builtin_amdgcn_raw_buffer_load_b128(rs, off, 0, AUX_SC1); }
__device__ __forceinline__ void mb_st(rsrc_t rs, unsigned off, v4u v) { __builtin_amdgcn_raw_buffer_store_b128(v, rs, off, 0, AUX_SC1); }
__device__ __forceinline__ void mb_st1(rsrc_t rs, unsigned off, unsigned v) { __builtin_amdgcn_raw_buffer_store_b32(v, rs, off, 0, AUX_SC1); }
__device__ __forceinline__ unsigned nos(float f) { unsigned u = __float_as_uint(f); return u < SENT ? u : SENT - 1u; }
__device__ __forceinline__ unsigned umax4(v4u v) { return max(max(v[0], v[1]), max(v[2], v[3])); }

__device__ __forceinline__ void wtile_load(v4u (&w)[8], const u16* base, int ld, int lane) {
  const int n = lane & 15, kg = lane >> 4;
  const u16* p = base + (size_t)(32 * (n >> 3) + 8 * kg) * ld + 8 * (n & 7);
#pragma unroll
  for (int t = 0; t < 8; ++t) w[t] = ldnt(p + (size_t)t * ld);
}
__device__ __forceinline__ void wtile_mma(v4f (&acc)[8], v4u a, const v4u (&w)[8]) {
#pragma unroll
  for (int c = 0; c < 4; ++c) {
    v4u lo, hi;
#pragma unroll
    for (int u = 0; u < 4; ++u) {
      lo[u] = __builtin_amdgcn_perm(w[2 * u + 1][c], w[2 * u][c], 0x05040100u);
      hi[u] = __builtin_amdgcn_perm(w[2 * u + 1][c], w[2 * u][c], 0x07060302u);
    }
    acc[2 * c] = mfma(a, lo, acc[2 * c]);
    acc[2 * c + 1] = mfma(a, hi, acc[2 * c + 1]);
  }
}
__device__ __forceinline__ void wtile_out(const v4f (&acc)[8], int lane, v4u& o0, v4u& o1) {
  const bool ks = (lane & 8) != 0;
#pragma unroll
  for (int e = 0; e < 8; ++e) {
    const float send = ks ? acc[e][1] : acc[e][2];
    const float keep = ks ? acc[e][3] : acc[e][0];
    const unsigned v = nos(keep + dpp<ROR8>(send));
    if (e < 4) o0[e] = v; else o1[e - 4] = v;
  }
}

typedef __attribute__((address_space(3))) unsigned lds_u32;
__device__ __forceinline__ void lds_set(unsigned char* lds, int word) {
  asm volatile("s_waitcnt lgkmcnt(0)" ::: "memory");
  __hip_atomic_store((lds_u32*)(lds + L_W) + word, 1u, __ATOMIC_RELAXED, __HIP_MEMORY_SCOPE_WORKGROUP);
}
__device__ __forceinline__ void lds_wait(unsigned char* lds, int word) {
  for (unsigned spin = 0; spin < (SPIN_CAP << 4); ++spin) {
    const unsigned v = __hip_atomic_load((lds_u32*)(lds + L_W) + word, __ATOMIC_RELAXED, __HIP_MEMORY_SCOPE_WORKGROUP);
    if (__builtin_amdgcn_readfirstlane(v) != 0u) break;
    __builtin_amdgcn_s_sleep(1);
  }
  asm volatile("" ::: "memory");
}
__device__ __forceinline__ void ln_frags(const v4u (&xr)[8], v4u& a0, v4u& a1) {
  v2f xv[32];
#pragma unroll
  for (int c = 0; c < 8; ++c)
#pragma unroll
    for (int k = 0; k < 4; ++k) xv[4 * c + k] = v2f{bflo(xr[c][k]), bfhi(xr[c][k])};
  v2f s0 = xv[0] + xv[1], s1 = xv[2] + xv[3];
#pragma unroll
  for (int i = 4; i < 32; i += 2) { s0 += xv[i]; s1 += xv[i + 1]; }
  s0 += s1;
  float s = s0[0] + s0[1];
  s += dpp<XOR1>(s); s = xrow_sum(s);
  const float mean = s * (1.f / DM);
  const v2f m2 = {mean, mean};
  v2f q0 = {0.f, 0.f}, q1 = {0.f, 0.f};
#pragma unroll
  for (int i = 0; i < 32; i += 2) { const v2f d0 = xv[i] - m2, d1 = xv[i + 1] - m2; q0 = __builtin_elementwise_fma(d0, d0, q0); q1 = __builtin_elementwise_fma(d1, d1, q1); }
  q0 += q1;
  float q = q0[0] + q0[1];
  q += dpp<XOR1>(q); q = xrow_sum(q);
  const float var = q * (1.f / DM);
  const float mb = rbf(mean);
  const float den = rbf(sqrtf(rbf(rbf(var) + 1e-5f)));
  const float r = 1.f / den;
  float o[16];
#pragma unroll
  for (int k = 0; k < 16; ++k) {
    const float num = rbf(xv[k >> 1][k & 1] - mb);
    const float q0_ = num * r;
    o[k] = fmaf(fmaf(-q0_, den, num), r, q0_);
  }
#pragma unroll
  for (int k = 0; k < 4; ++k) { a0[k] = pk2(o[2 * k], o[2 * k + 1]); a1[k] = pk2(o[8 + 2 * k], o[8 + 2 * k + 1]); }
}
__device__ __forceinline__ void cnt_add(unsigned char* ws, unsigned off) {
  __hip_atomic_fetch_add((unsigned*)(ws + off), 1u, __ATOMIC_RELAXED, __HIP_MEMORY_SCOPE_AGENT);
}
__device__ __forceinline__ unsigned cnt_peek(unsigned char* ws, unsigned off) {
  unsigned r; const unsigned z = 0u;
  asm volatile("global_atomic_add %0, %1, %2, %3 sc0\n\ts_waitcnt vmcnt(0)" : "=&v"(r) : "v"(off), "v"(z), "s"(ws) : "memory");
  return r;
}

struct Params {
  const u16* x; const u16* kc; const u16* vc; const u16* wq; const u16* wo; const u16* wg; const u16* wu; const u16* wd;
  u16* out; unsigned char* ws;
};

#define STAMP(i) do {} while (0)
#define STAMP_FLUSH() do {} while (0)

__global__ void __launch_bounds__(NTHR, 3) fused_decode(Params p) {
  extern __shared__ __attribute__((aligned(16))) unsigned char lds[];
  const int tid = threadIdx.x, lane = tid & 63;
  const int wave = __builtin_amdgcn_readfirstlane(tid >> 6);
  STAMP(0);
  const int w = blockIdx.x, g = w & 7, j = w >> 3;
  const rsrc_t rs = __builtin_amdgcn_make_buffer_rsrc(p.ws, 0, WS_END, 0x00020000);
  const int n16 = lane & 15, x4 = lane >> 4;
  const int ob = 2 * x4 + (n16 >> 3), oco = n16 & 7;
  const int ab = 2 * (n16 >> 2) + ((n16 & 3) >> 1), aks = n16 & 1;
  const int q_kq = j >> 3, q_cb = j & 7;
  const bool has_f = w < 224;
  const int f_mat = w / 112, f_r = w % 112, f_kq = f_r / 28, f_cb = f_r % 28;
  const int d_rb = w >> 3, d_cb = w & 7;
  const int a_b = j >> 2, a_s = j & 3;
  const int o_h = j >> 3, o_cb = j & 7;
  const unsigned off_qp = WS_QP + (((g * 8 + ob) * 4 + q_kq) * 8 + q_cb) * 256 + oco * 16;
  const unsigned off_gu = WS_GU + (((f_mat * 4 + f_kq) * 8 + ob) * 28 + f_cb) * 256 + oco * 16;
  const unsigned off_dp = WS_DP + ((((ob * 8 + d_cb) * 2 + (oco >> 2)) * 2) * 32 + d_rb) * 64 + (oco & 3) * 16;
  const unsigned off_sp0 = WS_SP + ((((ob * 8 + o_cb) * 2 + (oco >> 2)) * 2) * 64 + (4 * g + o_h) * 2) * 64 + (oco & 3) * 16;
  const unsigned off_op = WS_OP + ((((g * 4 + x4) * 8 + a_b) * 4 + a_s) * 2 + (n16 >> 3)) * 256 + oco * 16;
  const unsigned off_lse = WS_LSE + (((g * 4 + x4) * 8 + a_b) * 4 + a_s) * 4;
  constexpr float LOG2E = 1.4426950408889634f;
  const v4u S4 = {SENT, SENT, SENT, SENT};

  if (wave >= W_A0) {
    const int a = wave - W_A0;
    const size_t kvbase = ((size_t)(a_b * HKV + g) * SK + (SK - WIN) + a_s * 256 + a * 32) * DH;
    v4u kf[8], vf[8], w0[8];
    const u16* kp = p.kc + kvbase + (n16 >> 2) * DH + x4 * 32 + (n16 & 3) * 8;
    const u16* vp = p.vc + kvbase + lane * 8;
    SCHED_FENCE();
    BARRIER();
    SCHED_FENCE();
#pragma unroll
    for (int t = 0; t < 8; ++t) kf[t] = ldnt(kp + t * 4 * DH);
#pragma unroll
    for (int t = 0; t < 8; ++t) vf[t] = ldnt(vp + t * 4 * DH);
    const int tt = a - 6;
    if (tt >= 0) wtile_load(w0, p.wo + (size_t)((4 * g + o_h) * 128 + 64 * tt) * DM + 64 * o_cb, DM, lane);
    SCHED_FENCE();
    STAMP(1);
    lds_wait(lds, SW_QREADY);
    STAMP(2);
    const v4u qf = *(const v4u*)(lds + L_Q + lane * 16);
    float sc[8];
#pragma unroll
    for (int t = 0; t < 8; ++t) {
      v4f d = mfma(kf[t], qf, v4f{0.f, 0.f, 0.f, 0.f});
      const int cl = lane & 3;
      float v = (cl & 2) ? ((cl & 1) ? d[3] : d[2]) : ((cl & 1) ? d[1] : d[0]);
      v += dpp<XOR1>(v); v += dpp<XOR2>(v);
      sc[t] = rbf(rbf(v) * 0.08838834764831845f);
    }
    float mx = fmaxf(fmaxf(fmaxf(sc[0], sc[1]), fmaxf(sc[2], sc[3])), fmaxf(fmaxf(sc[4], sc[5]), fmaxf(sc[6], sc[7])));
    mx = xrow_max(mx);
    float pr[8], ls = 0.f;
#pragma unroll
    for (int t = 0; t < 8; ++t) { pr[t] = __builtin_amdgcn_exp2f((sc[t] - mx) * LOG2E); ls += pr[t]; }
    ls = xrow_sum(ls);
    if (lane < 16 && (lane & 3) == 0) {
      *(float*)(lds + L_MX + ((n16 >> 2) * 8 + a) * 4) = mx;
      *(float*)(lds + L_LS + ((n16 >> 2) * 8 + a) * 4) = ls;
    }
    const v4u pf = {pk2(pr[0], pr[1]), pk2(pr[2], pr[3]), pk2(pr[4], pr[5]), pk2(pr[6], pr[7])};
    STAMP(3);
    v4f acc[8];
#pragma unroll
    for (int e = 0; e < 8; ++e) acc[e] = v4f{0.f, 0.f, 0.f, 0.f};
    wtile_mma(acc, pf, vf);
    *(v4f*)(lds + L_O + a * 2048 + lane * 32) = v4f{acc[0][0], acc[1][0], acc[2][0], acc[3][0]};
    *(v4f*)(lds + L_O + a * 2048 + lane * 32 + 16) = v4f{acc[4][0], acc[5][0], acc[6][0], acc[7][0]};
    asm volatile("s_waitcnt lgkmcnt(0)" ::: "memory");
    unsigned old = 0;
    if (lane == 0) old = __hip_atomic_fetch_add((lds_u32*)(lds + L_W) + SW_ARRIVE, 1u, __ATOMIC_RELAXED, __HIP_MEMORY_SCOPE_WORKGROUP);
    old = __builtin_amdgcn_readfirstlane(old);
    asm volatile("" ::: "memory");
    STAMP(4);
    if (old == 7u) {
      const v4f m0 = *(const v4f*)(lds + L_MX + x4 * 32), m1 = *(const v4f*)(lds + L_MX + x4 * 32 + 16);
      const v4f l0 = *(const v4f*)(lds + L_LS + x4 * 32), l1 = *(const v4f*)(lds + L_LS + x4 * 32 + 16);
      const float M = fmaxf(fmaxf(fmaxf(m0[0], m0[1]), fmaxf(m0[2], m0[3])), fmaxf(fmaxf(m1[0], m1[1]), fmaxf(m1[2], m1[3])));
      float wt[8], L = 0.f;
#pragma unroll
      for (int ww = 0; ww < 8; ++ww) {
        wt[ww] = __builtin_amdgcn_exp2f(((ww < 4 ? m0[ww & 3] : m1[ww & 3]) - M) * LOG2E);
        L = fmaf(wt[ww], ww < 4 ? l0[ww & 3] : l1[ww & 3], L);
      }
      v4f o0 = {0.f, 0.f, 0.f, 0.f}, o1 = {0.f, 0.f, 0.f, 0.f};
#pragma unroll
      for (int ww = 0; ww < 8; ++ww) {
        const v4f a0 = *(const v4f*)(lds + L_O + ww * 2048 + lane * 32), a1 = *(const v4f*)(lds + L_O + ww * 2048 + lane * 32 + 16);
        const v4f wv = {wt[ww], wt[ww], wt[ww], wt[ww]};
        o0 = __builtin_elementwise_fma(wv, a0, o0); o1 = __builtin_elementwise_fma(wv, a1, o1);
      }
      const float inv = 1.f / L;
      lds_wait(lds, SW_FLAGOK);
      mb_st(rs, off_op, v4u{nos(o0[0] * inv), nos(o0[1] * inv), nos(o0[2] * inv), nos(o0[3] * inv)});
      mb_st(rs, off_op + 128, v4u{nos(o1[0] * inv), nos(o1[1] * inv), nos(o1[2] * inv), nos(o1[3] * inv)});
      if (n16 == 0) mb_st1(rs, off_lse, nos(fmaf(M, LOG2E, __builtin_amdgcn_logf(L))));
      if (lane < 16) cnt_add(p.ws, WS_C2 + ((g * 16 + lane) * 4 + a_s) * 128);
      lds_set(lds, SW_ATTDONE);
      STAMP(5);
    }
    if (tt >= 0) {
      const unsigned oo = WS_OP + (((g * 4 + o_h) * 8 + ab) * 8 + tt) * 256 + (4 * aks + x4) * 16;
      const unsigned lo = WS_LSE + ((g * 4 + o_h) * 8 + ab) * 16;
      v4u v[8], lv;
      lds_wait(lds, SW_FLAGOK);
      if (lane < 4) {
        const unsigned co = WS_C2 + ((g * 16 + (j >> 1)) * 4 + lane) * 128;
        for (unsigned spin = 0; spin < SPIN_CAP; ++spin) { if (__all(cnt_peek(p.ws, co) == 8u)) break; }
      }
      for (unsigned spin = 0; spin < SPIN_CAP; ++spin) {
        asm volatile("" ::: "memory");
        lv = mb_ld(rs, lo);
#pragma unroll
        for (int k = 0; k < 4; ++k) { v[2 * k] = mb_ld(rs, oo + k * 512); v[2 * k + 1] = mb_ld(rs, oo + k * 512 + 128); }
        unsigned m = umax4(lv);
#pragma unroll
        for (int k = 0; k < 8; ++k) m = max(m, umax4(v[k]));
        if (__all(m != SENT)) break;
      }
      STAMP(6);
      const float l0 = __uint_as_float(lv[0]), l1 = __uint_as_float(lv[1]), l2 = __uint_as_float(lv[2]), l3 = __uint_as_float(lv[3]);
      const float lm = fmaxf(fmaxf(l0, l1), fmaxf(l2, l3));
      float e0 = __builtin_amdgcn_exp2f(l0 - lm), e1 = __builtin_amdgcn_exp2f(l1 - lm), e2 = __builtin_amdgcn_exp2f(l2 - lm), e3 = __builtin_amdgcn_exp2f(l3 - lm);
      const float inv = 1.f / ((e0 + e1) + (e2 + e3));
      e0 *= inv; e1 *= inv; e2 *= inv; e3 *= inv;
      float o[8];
#pragma unroll
      for (int c = 0; c < 8; ++c) {
        const int hh = c >> 2, e = c & 3;
        o[c] = fmaf(e0, __uint_as_float(v[hh][e]), e1 * __uint_as_float(v[2 + hh][e])) + fmaf(e2, __uint_as_float(v[4 + hh][e]), e3 * __uint_as_float(v[6 + hh][e]));
      }
      const v4u af = {pk2(o[0], o[1]), pk2(o[2], o[3]), pk2(o[4], o[5]), pk2(o[6], o[7])};
#pragma unroll
      for (int e = 0; e < 8; ++e) acc[e] = v4f{0.f, 0.f, 0.f, 0.f};
      wtile_mma(acc, af, w0);
      v4u o0, o1; wtile_out(acc, lane, o0, o1);
      mb_st(rs, off_sp0 + tt * 64, o0); mb_st(rs, off_sp0 + tt * 64 + 4096, o1);
      if (lane < 8) cnt_add(p.ws, WS_C3 + ((o_cb * 8 + lane) * 12 + g) * 128);
      STAMP(7);
    }
  } else if (wave == W_C) {
    const v2u e = __builtin_amdgcn_raw_buffer_load_b64(rs, WS_EPOCH, 0, AUX_SC1);
    mb_st(rs, off_qp, S4); mb_st(rs, off_qp + 128, S4);
    if (lane < 8) *(unsigned*)(lds + L_W + lane * 4) = 0u;
    SCHED_FENCE();
    BARRIER();
    SCHED_FENCE();
    const u64 T = (((u64)e[1] << 32) | e[0]) + EPOCH_STEP;
    const unsigned tl = (unsigned)T, th = (unsigned)(T >> 32);
    asm volatile("s_waitcnt vmcnt(0)" ::: "memory");
    if (lane == 0) { __builtin_amdgcn_raw_buffer_store_b64(v2u{tl, th}, rs, WS_FLAG1 + (g * 32 + j) * 8, 0, AUX_SC1); *(v2u*)(lds + L_T) = v2u{tl, th}; }
    lds_set(lds, SW_CLEARED);
    STAMP(1);
    const int fcol = 64 * ((w >> 2) & 7) + 8 * (4 * ((w >> 1) & 1) + (lane & 3)) + 4 * (w & 1);
    const v2u xres = *(const v2u*)(p.x + (w >> 5) * DM + fcol);
    __builtin_amdgcn_s_sleep(F1_DELAY);
    for (unsigned spin = 0; spin < SPIN_CAP; ++spin) {
      asm volatile("" ::: "memory");
      const v4u f0 = mb_ld(rs, WS_FLAG1 + g * 256 + n16 * 16);
      const bool ok = f0[0] == tl && f0[1] == th && f0[2] == tl && f0[3] == th;
      if (__all(ok)) break;
      __builtin_amdgcn_s_sleep(4);
    }
    STAMP(2);
    {
      const unsigned qo = WS_QP + ((g * 8 + a_b) * 32 + 2 * (n16 >> 2) + (x4 >> 1)) * 256 + (4 * (x4 & 1) + (n16 & 3)) * 16;
      v4u v[8];
      for (unsigned spin = 0; spin < SPIN_CAP; ++spin) {
        asm volatile("" ::: "memory");
#pragma unroll
        for (int k = 0; k < 4; ++k) { v[2 * k] = mb_ld(rs, qo + k * 2048); v[2 * k + 1] = mb_ld(rs, qo + k * 2048 + 128); }
        unsigned m = 0;
#pragma unroll
        for (int k = 0; k < 8; ++k) m = max(m, umax4(v[k]));
        if (__all(m != SENT)) break;
        __builtin_amdgcn_s_sleep(4);
      }
      float q[8];
#pragma unroll
      for (int c = 0; c < 4; ++c) {
        q[c] = (__uint_as_float(v[0][c]) + __uint_as_float(v[2][c])) + (__uint_as_float(v[4][c]) + __uint_as_float(v[6][c]));
        q[4 + c] = (__uint_as_float(v[1][c]) + __uint_as_float(v[3][c])) + (__uint_as_float(v[5][c]) + __uint_as_float(v[7][c]));
      }
      *(v4u*)(lds + L_Q + lane * 16) = v4u{pk2(q[0], q[1]), pk2(q[2], q[3]), pk2(q[4], q[5]), pk2(q[6], q[7])};
    }
    lds_set(lds, SW_QREADY);
    STAMP(3);
    lds_wait(lds, SW_ATTDONE);
    STAMP(4);
    {
      const unsigned so = WS_SP + w * 4096 + lane * 16, dof = WS_DP + w * 2048 + lane * 16;
      const bool d1live = lane < 48;
      v4u s[4], d[2];
      if (lane < 12) {
        const unsigned co = WS_C3 + ((((w >> 2) & 7) * 8 + (w >> 5)) * 12 + lane) * 128;
        const unsigned want = lane < 8 ? 8u : 7u;
        for (unsigned spin = 0; spin < SPIN_CAP; ++spin) { if (__all(cnt_peek(p.ws, co) == want)) break; }
      }
      STAMP(7);
      for (unsigned spin = 0; spin < SPIN_CAP; ++spin) {
        asm volatile("" ::: "memory");
#pragma unroll
        for (int k = 0; k < 4; ++k) s[k] = mb_ld(rs, so + k * 1024);
        d[0] = mb_ld(rs, dof);
        d[1] = mb_ld(rs, d1live ? dof + 1024 : dof);
        unsigned m = max(max(umax4(s[0]), umax4(s[1])), max(umax4(s[2]), umax4(s[3])));
        m = max(m, max(umax4(d[0]), umax4(d[1])));
        if (__all(m != SENT)) break;
      }
      STAMP(5);
      float r[4];
#pragma unroll
      for (int c = 0; c < 4; ++c) {
        float sv = (__uint_as_float(s[0][c]) + __uint_as_float(s[1][c])) + (__uint_as_float(s[2][c]) + __uint_as_float(s[3][c]));
        float dv = __uint_as_float(d[0][c]) + (d1live ? __uint_as_float(d[1][c]) : 0.f);
        sv += dpp<ROR4>(sv); sv += dpp<ROR8>(sv); sv = xrow_sum(sv);
        dv += dpp<ROR4>(dv); dv += dpp<ROR8>(dv); dv = xrow_sum(dv);
        sv = rbf(sv); dv = rbf(dv);
        const float gt = rbf(dv / (1.f + __expf(-dv)));
        r[c] = rbf(sv * gt);
      }
      if (lane < 4) {
        v2u o = {pk2(bflo(xres[0]) + r[0], bfhi(xres[0]) + r[1]), pk2(bflo(xres[1]) + r[2], bfhi(xres[1]) + r[3])};
        *(v2u*)(p.out + (w >> 5) * DM + fcol) = o;
      }
    }
    STAMP(6);
  } else if (wave == W_Q) {
    v4u xr[8], w0[8], w1[8];
    {
      const u16* xp = p.x + ab * DM + 32 * aks + 8 * x4;
#pragma unroll
      for (int c = 0; c < 8; ++c) xr[c] = *(const v4u*)(xp + 64 * ((c + 2 * q_kq) & 7));
      const u16* wb = p.wq + 512 * g + 64 * q_cb + (size_t)(128 * q_kq) * (HQ * DH);
      wtile_load(w0, wb, HQ * DH, lane);
      wtile_load(w1, wb + (size_t)64 * (HQ * DH), HQ * DH, lane);
    }
    SCHED_FENCE();
    BARRIER();
    SCHED_FENCE();
    STAMP(1);
    v4u a0, a1;
    ln_frags(xr, a0, a1);
    STAMP(2);
    v4f acc[8];
#pragma unroll
    for (int e = 0; e < 8; ++e) acc[e] = v4f{0.f, 0.f, 0.f, 0.f};
    wtile_mma(acc, a0, w0);
    wtile_mma(acc, a1, w1);
    v4u o0, o1; wtile_out(acc, lane, o0, o1);
    lds_wait(lds, SW_CLEARED);
    mb_st(rs, off_qp, o0); mb_st(rs, off_qp + 128, o1);
    STAMP(3);
  } else {
    v4u xr[8], w0[8], w1[8];
    SCHED_FENCE();
    BARRIER();
    SCHED_FENCE();
    if (has_f) { mb_st(rs, off_gu, S4); mb_st(rs, off_gu + 128, S4); mb_st(rs, off_dp, S4); mb_st(rs, off_dp + 2048, S4); }
    mb_st(rs, off_sp0, S4); mb_st(rs, off_sp0 + 4096, S4); mb_st(rs, off_sp0 + 64, S4); mb_st(rs, off_sp0 + 64 + 4096, S4);
    mb_st(rs, off_op, S4); mb_st(rs, off_op + 128, S4);
    if (n16 == 0) mb_st1(rs, off_lse, SENT);
    if (lane < 5) mb_st1(rs, WS_C2 + (w * 5 + lane) * 128, 0u);
    asm volatile("s_waitcnt vmcnt(0)" ::: "memory");
    lds_wait(lds, SW_CLEARED);
    const v2u T2 = *(const v2u*)(lds + L_T);
    const unsigned tl = T2[0], th = T2[1];
    if (lane == 0) __builtin_amdgcn_raw_buffer_store_b64(T2, rs, WS_FLAG2 + w * 8, 0, AUX_SC1);
    STAMP(1);
    v4f acc[8];
    v4u o0, o1;
    if (has_f) {
      const u16* xp = p.x + ab * DM + 32 * aks + 8 * x4;
#pragma unroll
      for (int c = 0; c < 8; ++c) xr[c] = *(const v4u*)(xp + 64 * ((c + 2 * f_kq) & 7));
      const u16* wb = (f_mat ? p.wu : p.wg) + 64 * f_cb + (size_t)(128 * f_kq) * FF;
      wtile_load(w0, wb, FF, lane);
      wtile_load(w1, wb + (size_t)64 * FF, FF, lane);
      SCHED_FENCE();
      v4u a0, a1;
      ln_frags(xr, a0, a1);
#pragma unroll
      for (int e = 0; e < 8; ++e) acc[e] = v4f{0.f, 0.f, 0.f, 0.f};
      wtile_mma(acc, a0, w0);
      wtile_mma(acc, a1, w1);
      wtile_out(acc, lane, o0, o1);
      mb_st(rs, off_gu, o0); mb_st(rs, off_gu + 128, o1);
    } else {
      __builtin_amdgcn_s_sleep(64);
    }
    STAMP(2);
    for (unsigned spin = 0; spin < SPIN_CAP; ++spin) {
      asm volatile("" ::: "memory");
      const v4u f0 = mb_ld(rs, WS_FLAG2 + lane * 16), f1 = mb_ld(rs, WS_FLAG2 + 1024 + lane * 16);
      const bool ok = f0[0] == tl && f0[1] == th && f0[2] == tl && f0[3] == th && f1[0] == tl && f1[1] == th && f1[2] == tl && f1[3] == th;
      if (__all(ok)) break;
      __builtin_amdgcn_s_sleep(16);
    }
    lds_set(lds, SW_FLAGOK);
    if (w == 0 && lane == 0) __builtin_amdgcn_raw_buffer_store_b64(T2, rs, WS_EPOCH, 0, AUX_SC1);
    STAMP(3);
    if (has_f) {
      const unsigned go = WS_GU + (ab * 28 + d_rb) * 256 + (4 * aks + x4) * 16;
      v4u v[16];
#pragma unroll
      for (int k = 0; k < 8; ++k) { v[2 * k] = mb_ld(rs, go + k * (8 * 28 * 256)); v[2 * k + 1] = mb_ld(rs, go + k * (8 * 28 * 256) + 128); }
      wtile_load(w0, p.wd + (size_t)(64 * d_rb) * DM + 64 * d_cb, DM, lane);
      SCHED_FENCE();
      for (unsigned spin = 0; spin < SPIN_CAP; ++spin) {
        unsigned m = 0;
#pragma unroll
        for (int k = 0; k < 16; ++k) m = max(m, umax4(v[k]));
        if (__all(m != SENT)) break;
        asm volatile("" ::: "memory");
        __builtin_amdgcn_s_sleep(8);
#pragma unroll
        for (int k = 0; k < 8; ++k) { v[2 * k] = mb_ld(rs, go + k * (8 * 28 * 256)); v[2 * k + 1] = mb_ld(rs, go + k * (8 * 28 * 256) + 128); }
      }
      STAMP(4);
      float h[8];
#pragma unroll
      for (int c = 0; c < 8; ++c) {
        const int o = c >> 2, e = c & 3;
        const float gv = (__uint_as_float(v[o][e]) + __uint_as_float(v[2 + o][e])) + (__uint_as_float(v[4 + o][e]) + __uint_as_float(v[6 + o][e]));
        const float uv = (__uint_as_float(v[8 + o][e]) + __uint_as_float(v[10 + o][e])) + (__uint_as_float(v[12 + o][e]) + __uint_as_float(v[14 + o][e]));
        h[c] = rbf(gv) * rbf(uv);
      }
      const v4u hf = {pk2(h[0], h[1]), pk2(h[2], h[3]), pk2(h[4], h[5]), pk2(h[6], h[7])};
#pragma unroll
      for (int e = 0; e < 8; ++e) acc[e] = v4f{0.f, 0.f, 0.f, 0.f};
      wtile_mma(acc, hf, w0);
      wtile_out(acc, lane, o0, o1);
      mb_st(rs, off_dp, o0); mb_st(rs, off_dp + 2048, o1);
      if (lane < 8) cnt_add(p.ws, WS_C3 + ((d_cb * 8 + lane) * 12 + 8 + (d_rb & 3)) * 128);
      STAMP(5);
    }
  }
  STAMP_FLUSH();
}

extern "C" void kernel_launch(void* const* d_in, const int* in_sizes, int n_in, void* d_out, int out_size, void* d_ws, size_t ws_size, hipStream_t stream) {
  Params p;
  p.x = (const u16*)d_in[0]; p.kc = (const u16*)d_in[1]; p.vc = (const u16*)d_in[2]; p.wq = (const u16*)d_in[3]; p.wo = (const u16*)d_in[4];
  p.wg = (const u16*)d_in[6]; p.wu = (const u16*)d_in[7]; p.wd = (const u16*)d_in[8];
  p.out = (u16*)d_out; p.ws = (unsigned char*)d_ws;
  (void)hipFuncSetAttribute((const void*)fused_decode, hipFuncAttributeMaxDynamicSharedMemorySize, LDS_BYTES);
  fused_decode<<<NWG, NTHR, LDS_BYTES, stream>>>(p);
}
```

```cpp
#include <hip/hip_runtime.h>
#include <hip/hip_bf16.h>
#include <stdint.h>

#define STAMPS 0
#define F1_DELAY 8
#define F1_GAP 8
#define TWIN 4
#define C2REP 16
#define Q_DELAY 8
#define GO_EARLY 1
#define G_DELAY 8
#define CODE_LINES 150

typedef unsigned short u16;
typedef unsigned long long u64;
typedef unsigned v4u __attribute__((ext_vector_type(4)));
typedef unsigned v2u __attribute__((ext_vector_type(2)));
typedef float v4f __attribute__((ext_vector_type(4)));
typedef __bf16 bf16x8 __attribute__((ext_vector_type(8)));
typedef __bf16 bf16x2 __attribute__((ext_vector_type(2)));
typedef float v2f __attribute__((ext_vector_type(2)));

constexpr int DM = 512, HQ = 32, HKV = 8, SK = 4096, DH = 128, WIN = 1024, FF = 1792;
constexpr int NWG = 256, NWAVE = 12, NTHR = NWAVE * 64;
constexpr int W_C = 0, W_Q = 1, W_F = 2, W_O = 3, W_K0 = 4, W_V0 = 8;

constexpr unsigned WS_EPOCH = 0;
constexpr unsigned WS_LSE = 8192;
constexpr unsigned WS_QP = 16384;
constexpr unsigned WS_GU = WS_QP + 8 * 8 * 4 * 8 * 256;
constexpr unsigned WS_OP = WS_GU + 2 * 4 * 8 * 28 * 256;
constexpr unsigned WS_SP = WS_OP + 8 * 4 * 8 * 4 * 2 * 256;
constexpr unsigned WS_DP = WS_SP + 256 * 64 * 64;
constexpr unsigned WS_C2 = WS_DP + 256 * 32 * 64;
constexpr unsigned WS_C3 = WS_C2 + 512 * 128;
constexpr unsigned WS_FIN = WS_C3 + 768 * 128;
constexpr unsigned WS_END = WS_FIN + 256 * 1024;
constexpr unsigned WS_STAMP = 8u << 20;

constexpr unsigned SENT = 0xFFFFFFFFu;
constexpr u64 EPOCH_STEP = 0x9E3779B97F4A7C15ull;
constexpr unsigned SPIN_CAP = 1u << 18;
constexpr int AUX_SC1 = 16, AUX_NT = 2;

constexpr int L_W = 0;
constexpr int L_T = 128;
constexpr int L_MX = 144;
constexpr int L_LS = L_MX + 64;
constexpr int L_Q = L_LS + 64;
constexpr int L_P = L_Q + 1024;
constexpr int L_O = L_P + 8192;
constexpr int L_END = L_O + 4 * 2048;
constexpr int LDS_BYTES = 96 * 1024;
static_assert(L_Q % 16 == 0 && L_END <= LDS_BYTES, "lds");
enum { SW_QISSUED, SW_QCLR, SW_TREADY, SW_QPUB, SW_GO, SW_FISSUED, SW_KISSUED, SW_VISSUED, SW_QREADY, SW_MAXES, SW_PREADY, SW_ARRIVE, SW_ATTDONE, SW_FLAGOK, SW_BCLR, SW_WODONE };

__device__ __forceinline__ float bflo(unsigned w) { return __uint_as_float(w << 16); }
__device__ __forceinline__ float bfhi(unsigned w) { return __uint_as_float(w & 0xffff0000u); }
__device__ __forceinline__ unsigned pk2(float lo, float hi) {
  v2f f = {lo, hi};
  bf16x2 b = __builtin_convertvector(f, bf16x2);
  return __builtin_bit_cast(unsigned, b);
}
__device__ __forceinline__ float rbf(float x) { return __uint_as_float(pk2(x, 0.f) << 16); }
__device__ __forceinline__ v4f mfma(v4u a, v4u b, v4f c) {
  return __builtin_amdgcn_mfma_f32_16x16x32_bf16(__builtin_bit_cast(bf16x8, a), __builtin_bit_cast(bf16x8, b), c, 0, 0, 0);
}
template <int CTRL> __device__ __forceinline__ float dpp(float x) {
  return __builtin_bit_cast(float, __builtin_amdgcn_mov_dpp(__builtin_bit_cast(int, x), CTRL, 0xf, 0xf, true));
}
constexpr int XOR1 = 0xB1, ROR8 = 0x128, ROR4 = 0x124;
__device__ __forceinline__ float swap16_add(float a, float b) {
  auto s = __builtin_amdgcn_permlane16_swap(__float_as_uint(a), __float_as_uint(b), false, false);
  return __uint_as_float(s[0]) + __uint_as_float(s[1]);
}
__device__ __forceinline__ float swap32_add(float a, float b) {
  auto s = __builtin_amdgcn_permlane32_swap(__float_as_uint(a), __float_as_uint(b), false, false);
  return __uint_as_float(s[0]) + __uint_as_float(s[1]);
}
__device__ __forceinline__ float xrow_sum(float x) { x = swap16_add(x, x); return swap32_add(x, x); }
__device__ __forceinline__ float xrow_max(float x) {
  auto s = __builtin_amdgcn_permlane16_swap(__float_as_uint(x), __float_as_uint(x), false, false);
  x = fmaxf(__uint_as_float(s[0]), __uint_as_float(s[1]));
  auto t = __builtin_amdgcn_permlane32_swap(__float_as_uint(x), __float_as_uint(x), false, false);
  return fmaxf(__uint_as_float(t[0]), __uint_as_float(t[1]));
}
#define BARRIER() do { asm volatile("s_waitcnt lgkmcnt(0)" ::: "memory"); __builtin_amdgcn_s_barrier(); asm volatile("" ::: "memory"); } while (0)
#define SCHED_FENCE() __builtin_amdgcn_sched_barrier(0)

typedef __amdgpu_buffer_rsrc_t rsrc_t;
__device__ __forceinline__ rsrc_t mkrs(const void* p, unsigned bytes) { return __builtin_amdgcn_make_buffer_rsrc((void*)p, 0, bytes, 0x00020000); }
__device__ __forceinline__ v4u mb_ld(rsrc_t rs, unsigned off) { return __builtin_amdgcn_raw_buffer_load_b128(rs, off, 0, AUX_SC1); }
__device__ __forceinline__ void mb_st(rsrc_t rs, unsigned off, v4u v) { __builtin_amdgcn_raw_buffer_store_b128(v, rs, off, 0, AUX_SC1); }
__device__ __forceinline__ void mb_st1(rsrc_t rs, unsigned off, unsigned v) { __builtin_amdgcn_raw_buffer_store_b32(v, rs, off, 0, AUX_SC1); }
__device__ __forceinline__ unsigned nos(float f) { unsigned u = __float_as_uint(f); return u < SENT ? u : SENT - 1u; }
__device__ __forceinline__ unsigned umax4(v4u v) { return max(max(v[0], v[1]), max(v[2], v[3])); }
__device__ __forceinline__ float uf(unsigned u) { return __uint_as_float(u); }

__device__ __forceinline__ void tile_tr(v4u (&bt)[8], const v4u (&w)[8]) {
#pragma unroll
  for (int u = 0; u < 4; ++u)
#pragma unroll
    for (int c = 0; c < 4; ++c) {
      bt[2 * c][u] = __builtin_amdgcn_perm(w[2 * u + 1][c], w[2 * u][c], 0x05040100u);
      bt[2 * c + 1][u] = __builtin_amdgcn_perm(w[2 * u + 1][c], w[2 * u][c], 0x07060302u);
    }
}
__device__ __forceinline__ void wtile_out(const v4f (&acc)[8], int lane, v4u& o0, v4u& o1) {
  const bool ks = (lane & 8) != 0;
#pragma unroll
  for (int e = 0; e < 8; ++e) {
    const float send = ks ? acc[e][1] : acc[e][2];
    const float keep = ks ? acc[e][3] : acc[e][0];
    const unsigned v = nos(keep + dpp<ROR8>(send));
    if (e < 4) o0[e] = v; else o1[e - 4] = v;
  }
}

typedef __attribute__((address_space(3))) unsigned lds_u32;
__device__ __forceinline__ void lds_set(unsigned char* lds, int word) {
  asm volatile("s_waitcnt lgkmcnt(0)" ::: "memory");
  __hip_atomic_store((lds_u32*)(lds + L_W) + word, 1u, __ATOMIC_RELAXED, __HIP_MEMORY_SCOPE_WORKGROUP);
}
__device__ __forceinline__ void lds_wait(unsigned char* lds, int word, unsigned want) {
#pragma nounroll
  for (unsigned spin = 0; spin < (SPIN_CAP << 4); ++spin) {
    const unsigned v = __hip_atomic_load((lds_u32*)(lds + L_W) + word, __ATOMIC_RELAXED, __HIP_MEMORY_SCOPE_WORKGROUP);
    if (__builtin_amdgcn_readfirstlane(v) >= want) break;
    __builtin_amdgcn_s_sleep(1);
  }
  asm volatile("" ::: "memory");
}
__device__ __forceinline__ unsigned lds_peek(unsigned char* lds, int word) {
  return __builtin_amdgcn_readfirstlane(__hip_atomic_load((lds_u32*)(lds + L_W) + word, __ATOMIC_RELAXED, __HIP_MEMORY_SCOPE_WORKGROUP));
}
__device__ __forceinline__ void lds_spin(unsigned char* lds, int word, unsigned want) {
#pragma nounroll
  for (unsigned spin = 0; spin < (SPIN_CAP << 5); ++spin) { if (lds_peek(lds, word) >= want) break; }
  asm volatile("" ::: "memory");
}
__device__ __forceinline__ unsigned lds_arrive(unsigned char* lds, int word, int lane) {
  asm volatile("s_waitcnt lgkmcnt(0)" ::: "memory");
  unsigned old = 0;
  if (lane == 0) old = __hip_atomic_fetch_add((lds_u32*)(lds + L_W) + word, 1u, __ATOMIC_RELAXED, __HIP_MEMORY_SCOPE_WORKGROUP);
  old = __builtin_amdgcn_readfirstlane(old);
  asm volatile("" ::: "memory");
  return old;
}
__device__ __forceinline__ void ln_frags(const v4u (&xr)[8], v4u& a0, v4u& a1) {
  const bf16x2 ones = __builtin_bit_cast(bf16x2, 0x3f803f80u);
  float sa = 0.f, sb = 0.f;
#pragma unroll
  for (int c = 0; c < 8; ++c)
#pragma unroll
    for (int k = 0; k < 4; k += 2) {
      const unsigned u0 = xr[c][k], u1 = xr[c][k + 1];
      sa = __builtin_amdgcn_fdot2_f32_bf16(__builtin_bit_cast(bf16x2, u0), ones, sa, false);
      sb = __builtin_amdgcn_fdot2_f32_bf16(__builtin_bit_cast(bf16x2, u1), ones, sb, false);
    }
  float s = sa + sb;
  s += dpp<XOR1>(s); s = xrow_sum(s);
  const float mean = s * (1.f / DM);
  const v2f m2 = {mean, mean};
  v2f q0 = {0.f, 0.f}, q1 = {0.f, 0.f};
#pragma unroll
  for (int c = 0; c < 8; ++c)
#pragma unroll
    for (int k = 0; k < 4; k += 2) {
      const v2f d0 = v2f{bflo(xr[c][k]), bfhi(xr[c][k])} - m2, d1 = v2f{bflo(xr[c][k + 1]), bfhi(xr[c][k + 1])} - m2;
      q0 = __builtin_elementwise_fma(d0, d0, q0); q1 = __builtin_elementwise_fma(d1, d1, q1);
    }
  q0 += q1;
  float q = q0[0] + q0[1];
  q += dpp<XOR1>(q); q = xrow_sum(q);
  const float var = q * (1.f / DM);
  const float mb = rbf(mean);
  const float den = rbf(sqrtf(rbf(rbf(var) + 1e-5f)));
  const float r = 1.f / den;
#pragma unroll
  for (int k = 0; k < 8; ++k) {
    const unsigned xw = xr[k >> 2][k & 3];
    const unsigned n2 = pk2(bflo(xw) - mb, bfhi(xw) - mb);
    const float n0 = bflo(n2), n1 = bfhi(n2);
    const float t0 = n0 * r, t1 = n1 * r;
    const unsigned o = pk2(fmaf(fmaf(-t0, den, n0), r, t0), fmaf(fmaf(-t1, den, n1), r, t1));
    if (k < 4) a0[k] = o; else a1[k - 4] = o;
  }
}
__device__ __forceinline__ void cnt_add(unsigned char* ws, unsigned off) {
  __hip_atomic_fetch_add((unsigned*)(ws + off), 1u, __ATOMIC_RELAXED, __HIP_MEMORY_SCOPE_AGENT);
}
#define CNT_PEEK(r) "v_mov_b32 " r ", 0\n\tbuffer_atomic_add " r ", %2, %3, 0 offen sc0"
__device__ __forceinline__ void cnt_wait(v4u srd, unsigned off, int lane, int n, unsigned want, unsigned& r0, unsigned& r1) {
  const bool idle = lane >= n;
  off = idle ? 0xfffffff0u : off;
  r1 = 0u;
  asm volatile(CNT_PEEK("%0") : "=&v"(r0), "+v"(r1) : "v"(off), "s"(srd) : "memory");
  __builtin_amdgcn_s_sleep(2);
#pragma nounroll
  for (unsigned spin = 0; spin < SPIN_CAP; ++spin) {
    asm volatile(CNT_PEEK("%1") "\n\ts_waitcnt vmcnt(1)" : "+v"(r0), "+v"(r1) : "v"(off), "s"(srd) : "memory");
    if (__all(idle || r0 == want)) break;
    asm volatile(CNT_PEEK("%0") "\n\ts_waitcnt vmcnt(1)" : "+v"(r0), "+v"(r1) : "v"(off), "s"(srd) : "memory");
    if (__all(idle || r1 == want)) break;
  }
}
#define CNT_KEEP(r0, r1) asm volatile("" :: "v"(r0), "v"(r1))
__device__ __forceinline__ v4u mksrd(const void* p, unsigned bytes) {
  const u64 a = (u64)p;
  return v4u{(unsigned)a, (unsigned)(a >> 32) & 0xffffu, bytes, 0x00020000u};
}

#define FLAG_PEEK(r) "v_mov_b64 " r ", 0\n\tbuffer_atomic_or_x2 " r ", %2, %3, 0 offen sc0"
template <int GAP> __device__ __forceinline__ void flag_wait(v4u srd, unsigned off, int lane, int n, v2u T, v2u& f0, v2u& f1) {
  const bool idle = lane >= n;
  off = idle ? 0xfffffff0u : off;
  f1 = v2u{0u, 0u};
  asm volatile(FLAG_PEEK("%0") : "=&v"(f0), "+v"(f1) : "v"(off), "s"(srd) : "memory");
#pragma nounroll
  for (unsigned spin = 0; spin < SPIN_CAP; ++spin) {
    __builtin_amdgcn_s_sleep(GAP);
    asm volatile(FLAG_PEEK("%1") "\n\ts_waitcnt vmcnt(1)" : "+v"(f0), "+v"(f1) : "v"(off), "s"(srd) : "memory");
    if (__all(idle || (f0[0] == T[0] && f0[1] == T[1]))) break;
    __builtin_amdgcn_s_sleep(GAP);
    asm volatile(FLAG_PEEK("%0") "\n\ts_waitcnt vmcnt(1)" : "+v"(f0), "+v"(f1) : "v"(off), "s"(srd) : "memory");
    if (__all(idle || (f1[0] == T[0] && f1[1] == T[1]))) break;
  }
}

struct Params {
  const u16* x; const u16* kc; const u16* vc; const u16* wq; const u16* wo; const u16* wg; const u16* wu; const u16* wd;
  u16* out; unsigned char* ws;
};

#define STAMP(i) do {} while (0)
#define STAMP_FLUSH() do {} while (0)

__global__ void __launch_bounds__(NTHR, 3) fused_decode(Params p) {
  extern __shared__ __attribute__((aligned(16))) unsigned char lds[];
  const int tid = threadIdx.x, lane = tid & 63;
  const int wave = __builtin_amdgcn_readfirstlane(tid >> 6);
  STAMP(0);
  u64 pc0; asm volatile("s_getpc_b64 %0" : "=s"(pc0));
  if (tid < 32) *(unsigned*)(lds + L_W + tid * 4) = 0u;
  BARRIER();
  const int w = blockIdx.x, g = w & 7, j = w >> 3;
  const int n16 = lane & 15, x4 = lane >> 4;
  const int ob = 2 * x4 + (n16 >> 3), oco = n16 & 7;
  const int ab = 2 * (n16 >> 2) + ((n16 & 3) >> 1), aks = n16 & 1;
  const int k4 = j >> 3, m8 = j & 7, ci = g * 4 + k4;
  const int q_kq = k4, q_cb = m8;
  const int f_t = k4 * 8 + g;
  const bool has_f = f_t < 28;
  const int f_mat = m8 >> 2, f_kq = m8 & 3;
  const int a_b = j >> 2, a_s = j & 3;
  const int o_h = k4;
  const bool is_q = wave == W_Q, is_f = wave == W_F, is_o = wave == W_O;
  constexpr float LOG2E = 1.4426950408889634f;
  const v4u S4 = {SENT, SENT, SENT, SENT};
  const unsigned wt_row = 32 * (n16 >> 3) + 8 * x4;

  if (wave >= W_K0) {
    unsigned char* ws = p.ws; asm volatile("; role A" : "+s"(ws));
    const rsrc_t rs = mkrs(ws, WS_END);
    const bool is_k = wave < W_V0;
    const int a = (wave - W_K0) & 3;
    const size_t kvb = ((size_t)(a_b * HKV + g) * SK + (SK - WIN) + a_s * 256 + a * 64) * (DH * 2);
    const unsigned off_op = WS_OP + ((((g * 4 + a) * 8 + a_b) * 4 + a_s) * 2 + (is_k ? 0 : 1)) * 256 + lane * 4;
    const unsigned char* osrc = lds + L_O + (lane >> 5) * 1024 + (2 * a + (is_k ? 0 : 1)) * 128 + (lane & 31) * 4;
    float inv;
    if (is_k) {
      v4u r[16];
      {
        const rsrc_t rs1 = mkrs((const unsigned char*)p.kc + kvb, 16384);
        const unsigned vo = (n16 >> 2) * 256 + x4 * 64 + (n16 & 3) * 16;
        SCHED_FENCE(); lds_wait(lds, SW_QISSUED, 1u); SCHED_FENCE();
        bool go = false;
#pragma unroll
        for (int t = 0; t < 16; ++t) {
          if (t >= TWIN) { if (!go) { go = lds_peek(lds, SW_FISSUED) != 0u; if (!go) asm volatile("s_waitcnt vmcnt(%0)" :: "i"(TWIN - 1) : "memory"); } SCHED_FENCE(); }
          r[t] = __builtin_amdgcn_raw_buffer_load_b128(rs1, vo + (t & 3) * 1024, (t >> 2) * 4096, AUX_NT);
          SCHED_FENCE();
        }
        lds_arrive(lds, SW_KISSUED, lane);
      }
      STAMP(1);
      lds_wait(lds, SW_QREADY, 1u);
      STAMP(2);
      const v4u qf = *(const v4u*)(lds + L_Q + lane * 16);
      float sc[16];
#pragma unroll
      for (int t = 0; t < 16; t += 2) {
        const v4f d = mfma(r[t], qf, v4f{0.f, 0.f, 0.f, 0.f});
        const v4f e = mfma(r[t + 1], qf, v4f{0.f, 0.f, 0.f, 0.f});
        float v0 = dpp<0x00>(d[0]); v0 += dpp<0x55>(d[1]); v0 += dpp<0xAA>(d[2]); v0 += dpp<0xFF>(d[3]);
        float v1 = dpp<0x00>(e[0]); v1 += dpp<0x55>(e[1]); v1 += dpp<0xAA>(e[2]); v1 += dpp<0xFF>(e[3]);
        const unsigned u0 = pk2(v0, v1);
        const unsigned u1 = pk2(bflo(u0) * 0.08838834764831845f, bfhi(u0) * 0.08838834764831845f);
        sc[t] = bflo(u1); sc[t + 1] = bfhi(u1);
      }
      float mx = sc[0];
#pragma unroll
      for (int t = 1; t < 16; ++t) mx = fmaxf(mx, sc[t]);
      mx = xrow_max(mx);
      const int hs = n16 >> 2;
      if (lane < 16 && (lane & 3) == 0) *(float*)(lds + L_MX + (hs * 4 + a) * 4) = mx;
      lds_arrive(lds, SW_MAXES, lane);
      lds_spin(lds, SW_MAXES, 4u);
      { const v4f m0 = *(const v4f*)(lds + L_MX + hs * 16); mx = fmaxf(fmaxf(m0[0], m0[1]), fmaxf(m0[2], m0[3])); }
      const float mxl = mx * LOG2E;
      float pr[16], ls = 0.f;
#pragma unroll
      for (int t = 0; t < 16; ++t) { pr[t] = __builtin_amdgcn_exp2f(fmaf(sc[t], LOG2E, -mxl)); ls += pr[t]; }
      ls = xrow_sum(ls);
      if (lane < 16 && (lane & 3) == 0) *(float*)(lds + L_LS + (hs * 4 + a) * 4) = ls;
      *(v4u*)(lds + L_P + (2 * a) * 1024 + lane * 16) = v4u{pk2(pr[0], pr[1]), pk2(pr[2], pr[3]), pk2(pr[4], pr[5]), pk2(pr[6], pr[7])};
      *(v4u*)(lds + L_P + (2 * a + 1) * 1024 + lane * 16) = v4u{pk2(pr[8], pr[9]), pk2(pr[10], pr[11]), pk2(pr[12], pr[13]), pk2(pr[14], pr[15])};
      lds_arrive(lds, SW_PREADY, lane);
      STAMP(3);
      lds_wait(lds, SW_PREADY, 4u);
      const v4f l0 = *(const v4f*)(lds + L_LS + a * 16), m0 = *(const v4f*)(lds + L_MX + a * 16);
      const float L = (l0[0] + l0[1]) + (l0[2] + l0[3]);
      inv = __builtin_amdgcn_rcpf(L);
      lds_wait(lds, SW_BCLR, 1u);
      if (lane == 0) mb_st1(rs, WS_LSE + (((g * 4 + a) * 8 + a_b) * 4 + a_s) * 4, nos(fmaf(fmaxf(fmaxf(m0[0], m0[1]), fmaxf(m0[2], m0[3])), LOG2E, __builtin_amdgcn_logf(L))));
      lds_wait(lds, SW_ARRIVE, 3u);
    } else {
      v4u r[16];
      {
        const rsrc_t rs2 = mkrs((const unsigned char*)p.vc + kvb, 16384);
        SCHED_FENCE(); lds_wait(lds, SW_KISSUED, 4u); lds_wait(lds, SW_FISSUED, 1u); SCHED_FENCE();
#pragma unroll
        for (int t = 0; t < 16; ++t) r[t] = __builtin_amdgcn_raw_buffer_load_b128(rs2, lane * 16 + (t & 3) * 1024, (t >> 2) * 4096, AUX_NT);
        SCHED_FENCE();
        lds_arrive(lds, SW_VISSUED, lane);
      }
      STAMP(1);
      lds_wait(lds, SW_PREADY, 4u);
      lds_wait(lds, SW_FLAGOK, 1u);
      STAMP(2);
      const v4u p0 = *(const v4u*)(lds + L_P + (2 * a) * 1024 + lane * 16), p1 = *(const v4u*)(lds + L_P + (2 * a + 1) * 1024 + lane * 16);
      { const v4f l0 = *(const v4f*)(lds + L_LS + a * 16); inv = __builtin_amdgcn_rcpf((l0[0] + l0[1]) + (l0[2] + l0[3])); }
      asm volatile("" : "+v"(inv));
      v4f acc[8];
#pragma unroll
      for (int h = 0; h < 2; ++h) {
        v4u bt[8];
#pragma unroll
        for (int u = 0; u < 4; ++u) {
          SCHED_FENCE();
#pragma unroll
          for (int c = 0; c < 4; ++c) {
            bt[2 * c][u] = __builtin_amdgcn_perm(r[8 * h + 2 * u + 1][c], r[8 * h + 2 * u][c], 0x05040100u);
            bt[2 * c + 1][u] = __builtin_amdgcn_perm(r[8 * h + 2 * u + 1][c], r[8 * h + 2 * u][c], 0x07060302u);
          }
        }
#pragma unroll
        for (int e = 0; e < 8; ++e) acc[e] = mfma(h ? p1 : p0, bt[e], h ? acc[e] : v4f{0.f, 0.f, 0.f, 0.f});
      }
      *(v4f*)(lds + L_O + a * 2048 + lane * 16) = v4f{acc[0][0], acc[1][0], acc[2][0], acc[3][0]};
      *(v4f*)(lds + L_O + a * 2048 + 1024 + lane * 16) = v4f{acc[4][0], acc[5][0], acc[6][0], acc[7][0]};
      lds_arrive(lds, SW_ARRIVE, lane);
      STAMP(3);
    }
    lds_spin(lds, SW_ARRIVE, 4u);
    STAMP(4);
    {
      const float o0 = *(const float*)(osrc), o1 = *(const float*)(osrc + 2048), o2 = *(const float*)(osrc + 4096), o3 = *(const float*)(osrc + 6144);
      mb_st1(rs, off_op, nos(((o0 + o1) + (o2 + o3)) * inv));
      if (wave == W_V0) {
        if (lane < C2REP) cnt_add(ws, WS_C2 + (g * 64 + lane) * 128);
        lds_set(lds, SW_ATTDONE);
      }
    }
    STAMP(5);
    STAMP_FLUSH();
    return;
  }

  if (wave == W_C) {
    unsigned char* ws = p.ws; asm volatile("; role C" : "+s"(ws));
    const rsrc_t rs = mkrs(ws, WS_END);
    const v4u srd = mksrd(ws, WS_END);
    const v2u ep = __builtin_amdgcn_raw_buffer_load_b64(rs, WS_EPOCH, 0, AUX_SC1);
    const int fidx = g * DM + 64 * m8 + 8 * (4 * (k4 >> 1) + (lane & 3)) + 4 * (k4 & 1) + x4;
    const float xres = bflo(p.x[fidx]);
    const u64 T64 = (((u64)ep[1] << 32) | ep[0]) + EPOCH_STEP;
    const v2u T = {(unsigned)T64, (unsigned)(T64 >> 32)};
    if (lane == 0) *(v2u*)(lds + L_T) = T;
    lds_set(lds, SW_TREADY);
    STAMP(1);
    lds_wait(lds, SW_QCLR, 1u);
    if (lane < 32) __builtin_amdgcn_raw_buffer_store_b64(T, rs, WS_FIN + (lane * 8 + g) * 1024 + 512 + j * 8, 0, AUX_SC1);
    STAMP(2);
    v2u f0, f1;
    __builtin_amdgcn_s_sleep(F1_DELAY);
    flag_wait<F1_GAP>(srd, WS_FIN + w * 1024 + 512 + lane * 8, lane, 32, T, f0, f1);
    STAMP(3);
    lds_wait(lds, SW_QPUB, 1u);
    __builtin_amdgcn_s_sleep(Q_DELAY);
    {
      const unsigned qo = WS_QP + ((g * 8 + a_b) * 32 + 2 * (n16 >> 2) + (x4 >> 1)) * 256 + (4 * (x4 & 1) + (n16 & 3)) * 16;
      v4u v[8];
#pragma nounroll
      for (unsigned spin = 0; spin < SPIN_CAP; ++spin) {
        asm volatile("" ::: "memory");
#pragma unroll
        for (int k = 0; k < 4; ++k) { v[2 * k] = mb_ld(rs, qo + k * 2048); v[2 * k + 1] = mb_ld(rs, qo + k * 2048 + 128); }
        SCHED_FENCE(); lds_set(lds, SW_GO); SCHED_FENCE();
        unsigned m = 0;
#pragma unroll
        for (int k = 0; k < 8; ++k) m = max(m, umax4(v[k]));
        if (__all(m != SENT)) break;
        __builtin_amdgcn_s_sleep(4);
      }
      CNT_KEEP(f0, f1);
      float q[8];
#pragma unroll
      for (int c = 0; c < 4; ++c) {
        q[c] = (uf(v[0][c]) + uf(v[2][c])) + (uf(v[4][c]) + uf(v[6][c]));
        q[4 + c] = (uf(v[1][c]) + uf(v[3][c])) + (uf(v[5][c]) + uf(v[7][c]));
      }
      *(v4u*)(lds + L_Q + lane * 16) = v4u{pk2(q[0], q[1]), pk2(q[2], q[3]), pk2(q[4], q[5]), pk2(q[6], q[7])};
    }
    lds_set(lds, SW_QREADY);
    lds_set(lds, SW_GO);
    STAMP(4);
    lds_wait(lds, SW_ATTDONE, 1u);
    const unsigned co = WS_C3 + ((m8 * 8 + g) * 12) * 128;
    float gate;
    {
      unsigned c0, c1;
      cnt_wait(srd, co + 8 * 128, lane, 1, 28u, c0, c1);
      const unsigned dof = WS_DP + w * 2048 + lane * 16;
      const bool d1live = lane < 48;
      v4u d0, d1;
#pragma nounroll
      for (unsigned spin = 0; spin < SPIN_CAP; ++spin) {
        asm volatile("" ::: "memory");
        d0 = mb_ld(rs, dof);
        d1 = mb_ld(rs, d1live ? dof + 1024 : dof);
        if (__all(max(umax4(d0), umax4(d1)) != SENT)) break;
      }
      CNT_KEEP(c0, c1);
      float dv[4];
#pragma unroll
      for (int c = 0; c < 4; ++c) dv[c] = uf(d0[c]) + (d1live ? uf(d1[c]) : 0.f);
      float t = swap32_add(swap16_add(dv[0], dv[1]), swap16_add(dv[2], dv[3]));
      t += dpp<ROR4>(t); t += dpp<ROR8>(t);
      t = rbf(t);
      gate = rbf(t / (1.f + __expf(-t)));
    }
    asm volatile("" : "+v"(gate));
    STAMP(5);
    {
      unsigned c0, c1;
      lds_wait(lds, SW_WODONE, 2u);
      cnt_wait(srd, co + lane * 128, lane, 4, 16u, c0, c1);
      STAMP(6);
      const unsigned so = WS_SP + w * 4096 + lane * 16;
      float t;
#pragma nounroll
      for (unsigned spin = 0; spin < SPIN_CAP; ++spin) {
        asm volatile("" ::: "memory");
        v4u s[4];
#pragma unroll
        for (int k = 0; k < 4; ++k) s[k] = mb_ld(rs, so + k * 1024);
        float sv[4];
#pragma unroll
        for (int c = 0; c < 4; ++c) sv[c] = (uf(s[0][c]) + uf(s[1][c])) + (uf(s[2][c]) + uf(s[3][c]));
        t = swap32_add(swap16_add(sv[0], sv[1]), swap16_add(sv[2], sv[3]));
        t += dpp<ROR4>(t); t += dpp<ROR8>(t);
        if (__all(t == t)) break;
        if (__all(max(max(umax4(s[0]), umax4(s[1])), max(umax4(s[2]), umax4(s[3]))) != SENT)) break;
      }
      CNT_KEEP(c0, c1);
      const float r = rbf(rbf(t) * gate);
      if (n16 < 4) p.out[fidx] = (u16)pk2(xres + r, 0.f);
      if (w == 0 && lane == 0) __builtin_amdgcn_raw_buffer_store_b64(T, rs, WS_EPOCH, 0, AUX_SC1);
    }
    STAMP(7);
    STAMP_FLUSH();
    return;
  }

  unsigned char* ws = p.ws; asm volatile("; role QFO" : "+s"(ws));
  const rsrc_t rs = mkrs(ws, WS_END);
  const unsigned off_gu = WS_GU + (((f_mat * 4 + f_kq) * 8 + ob) * 28 + f_t) * 256 + oco * 16;
  if (is_o) {
    unsigned pf0, pf1;
    const unsigned char* cp = (const unsigned char*)(pc0 & ~127ull) + lane * 128;
    asm volatile("global_load_dword %0, %2, off\n\tglobal_load_dword %1, %3, off" : "=&v"(pf0), "=&v"(pf1) : "v"(cp), "v"(cp + (CODE_LINES - 64) * 128) : "memory");
    lds_wait(lds, SW_QISSUED, 1u);
    const unsigned off_op = WS_OP + ((((g * 4 + x4) * 8 + a_b) * 4 + a_s) * 2 + (n16 >> 3)) * 256 + oco * 16;
    if (has_f) { mb_st(rs, off_gu, S4); mb_st(rs, off_gu + 128, S4); }
    mb_st(rs, off_op, S4); mb_st(rs, off_op + 128, S4);
#pragma unroll
    for (int k = 0; k < 4; ++k) mb_st(rs, WS_SP + w * 4096 + k * 1024 + lane * 16, S4);
    mb_st(rs, WS_DP + w * 2048 + lane * 16, S4); mb_st(rs, WS_DP + w * 2048 + 1024 + lane * 16, S4);
    const unsigned so = lane < 4 ? WS_LSE + (((g * 4 + lane) * 8 + a_b) * 4 + a_s) * 4
                      : lane < 6 ? WS_C2 + (g * 64 + 2 * j + (lane - 4)) * 128 : WS_C3 + (m8 * 96 + 3 * ci + (lane - 6)) * 128;
    if (lane < 9) mb_st1(rs, so, lane < 4 ? SENT : 0u);
    const int l5 = lane & 31;
    const unsigned fdst = WS_FIN + (lane < 32 ? (l5 * 8 + g) * 1024 + j * 8 : ((((l5 & 3) * 8 + m8) * 8) + (l5 >> 2)) * 1024 + (32 + ci) * 8);
    asm volatile("s_waitcnt vmcnt(0)" : "+v"(pf0), "+v"(pf1) :: "memory");
    lds_set(lds, SW_BCLR);
    lds_wait(lds, SW_TREADY, 1u);
    const v2u T = *(const v2u*)(lds + L_T);
    __builtin_amdgcn_raw_buffer_store_b64(T, rs, fdst, 0, AUX_SC1);
    STAMP(1);
    v2u f0, f1;
    __builtin_amdgcn_s_sleep(16);
    flag_wait<16>(mksrd(ws, WS_END), WS_FIN + w * 1024 + lane * 8, lane, 64, T, f0, f1);
    lds_set(lds, SW_FLAGOK);
    asm volatile("s_waitcnt vmcnt(0)" : "+v"(f0), "+v"(f1) :: "memory");
    STAMP(2);
  } else {
    v4u r1[8], r2[8], r3[8];
    const unsigned off_a = is_q ? WS_QP + (((g * 8 + ob) * 4 + q_kq) * 8 + q_cb) * 256 + oco * 16 : off_gu;
    {
      const unsigned stride = is_q ? HQ * DH * 2 : FF * 2;
      const int kq = is_q ? q_kq : f_kq;
      const unsigned char* b1 = (is_q ? (const unsigned char*)(p.wq + 512 * g + 64 * q_cb) : (const unsigned char*)((f_mat ? p.wu : p.wg) + 64 * f_t)) + (size_t)(128 * kq) * stride;
      const rsrc_t rs1 = mkrs(b1, (is_q || has_f) ? 127 * stride + 128 : 0u);
      const unsigned vo1 = wt_row * stride + oco * 16;
      const rsrc_t rsx = mkrs(p.x, 8 * DM * 2);
      const unsigned xo = ab * (DM * 2) + (32 * aks + 8 * x4) * 2;
      SCHED_FENCE();
      if (is_q) { mb_st(rs, off_a, S4); mb_st(rs, off_a + 128, S4); } else lds_wait(lds, SW_GO, 1u);
      SCHED_FENCE();
#pragma unroll
      for (int c = 0; c < 8; ++c) r3[c] = __builtin_amdgcn_raw_buffer_load_b128(rsx, xo, ((c + 2 * kq) & 7) * 128, 0);
#pragma unroll
      for (int t = 0; t < 8; ++t) r1[t] = __builtin_amdgcn_raw_buffer_load_b128(rs1, vo1, t * stride, AUX_NT);
#pragma unroll
      for (int t = 0; t < 8; ++t) r2[t] = __builtin_amdgcn_raw_buffer_load_b128(rs1, vo1, (64 + t) * stride, AUX_NT);
      SCHED_FENCE();
      lds_set(lds, is_q ? SW_QISSUED : SW_FISSUED);
      SCHED_FENCE();
    }
    STAMP(1);
    if (is_q) { asm volatile("s_waitcnt vmcnt(24)" ::: "memory"); lds_set(lds, SW_QCLR); }
    v4f acc[8];
    v4u o0, o1, bt[8];
    if (is_q || has_f) {
      v4u a0, a1;
      ln_frags(r3, a0, a1);
      STAMP(2);
      tile_tr(bt, r1);
#pragma unroll
      for (int e = 0; e < 8; ++e) acc[e] = mfma(a0, bt[e], v4f{0.f, 0.f, 0.f, 0.f});
      tile_tr(bt, r2);
#pragma unroll
      for (int e = 0; e < 8; ++e) acc[e] = mfma(a1, bt[e], acc[e]);
      wtile_out(acc, lane, o0, o1);
      if (is_f) lds_wait(lds, SW_BCLR, 1u);
      mb_st(rs, off_a, o0); mb_st(rs, off_a + 128, o1);
      if (is_q) lds_set(lds, SW_QPUB);
    }
    STAMP(3);
    if (is_f) {
      if (has_f) {
        lds_wait(lds, SW_FLAGOK, 1u);
        __builtin_amdgcn_s_sleep(G_DELAY);
        const unsigned go = WS_GU + (ab * 28 + f_t) * 256 + (4 * aks + x4) * 16;
        v4u v[16];
#pragma unroll
        for (int k = 0; k < 8; ++k) { v[2 * k] = mb_ld(rs, go + k * (8 * 28 * 256)); v[2 * k + 1] = mb_ld(rs, go + k * (8 * 28 * 256) + 128); }
        {
          const rsrc_t rs3 = mkrs(p.wd + (size_t)(64 * f_t) * DM + 64 * m8, 63 * 1024 + 128);
#pragma unroll
          for (int t = 0; t < 8; ++t) r1[t] = __builtin_amdgcn_raw_buffer_load_b128(rs3, wt_row * 1024 + oco * 16, t * 1024, AUX_NT);
        }
        SCHED_FENCE();
#pragma nounroll
        for (unsigned spin = 0; spin < SPIN_CAP; ++spin) {
          unsigned m = 0;
#pragma unroll
          for (int k = 0; k < 16; ++k) m = max(m, umax4(v[k]));
          if (__all(m != SENT)) break;
          asm volatile("" ::: "memory");
          __builtin_amdgcn_s_sleep(8);
#pragma unroll
          for (int k = 0; k < 8; ++k) { v[2 * k] = mb_ld(rs, go + k * (8 * 28 * 256)); v[2 * k + 1] = mb_ld(rs, go + k * (8 * 28 * 256) + 128); }
        }
        STAMP(4);
        float h[8];
#pragma unroll
        for (int c = 0; c < 8; ++c) {
          const int o = c >> 2, e = c & 3;
          const float gv = (uf(v[o][e]) + uf(v[2 + o][e])) + (uf(v[4 + o][e]) + uf(v[6 + o][e]));
          const float uv = (uf(v[8 + o][e]) + uf(v[10 + o][e])) + (uf(v[12 + o][e]) + uf(v[14 + o][e]));
          const unsigned gu = pk2(gv, uv);
          h[c] = bflo(gu) * bfhi(gu);
        }
        const v4u hf = {pk2(h[0], h[1]), pk2(h[2], h[3]), pk2(h[4], h[5]), pk2(h[6], h[7])};
        tile_tr(bt, r1);
#pragma unroll
        for (int e = 0; e < 8; ++e) acc[e] = mfma(hf, bt[e], v4f{0.f, 0.f, 0.f, 0.f});
        wtile_out(acc, lane, o0, o1);
        const unsigned off_dp = WS_DP + ((((oco >> 2) * 16 + m8) * 8 + ob) * 32 + f_t) * 64 + (oco & 3) * 16;
        mb_st(rs, off_dp, o0); mb_st(rs, off_dp + 64 * 2048, o1);
        if (lane < 8) cnt_add(ws, WS_C3 + ((m8 * 8 + lane) * 12 + 8) * 128);
        STAMP(5);
      }
      STAMP_FLUSH();
      return;
    }
  }
  {
    const int tt = is_o;
    v4u bt[8], r3[8]; v4f acc[8];
    lds_wait(lds, SW_VISSUED, 4u);
    {
      const rsrc_t rs3 = mkrs(p.wo + (size_t)((4 * g + o_h) * 128 + 64 * tt) * DM + 64 * m8, 63 * 1024 + 128);
#pragma unroll
      for (int t = 0; t < 8; ++t) r3[t] = __builtin_amdgcn_raw_buffer_load_b128(rs3, wt_row * 1024 + oco * 16, t * 1024, AUX_NT);
    }
    tile_tr(bt, r3);
#pragma unroll
    for (int e = 0; e < 8; ++e) asm volatile("" : "+v"(bt[e]));
    const unsigned oo = WS_OP + (((g * 4 + o_h) * 8 + ab) * 8 + tt) * 256 + (4 * aks + x4) * 16;
    const unsigned lo = WS_LSE + ((g * 4 + o_h) * 8 + ab) * 16;
    const unsigned off_sp = WS_SP + ((((oco >> 2) * 16 + m8) * 8 + ob) * 64 + (4 * g + o_h) * 2 + tt) * 64 + (oco & 3) * 16;
    STAMP(5);
    lds_wait(lds, SW_ATTDONE, 1u);
    unsigned c0, c1;
    cnt_wait(mksrd(ws, WS_END), WS_C2 + (g * 64 + (j & (C2REP - 1))) * 128, lane, 1, 32u, c0, c1);
    STAMP(4);
    v4f oa, obv;
#pragma nounroll
    for (unsigned spin = 0; spin < SPIN_CAP; ++spin) {
      asm volatile("" ::: "memory");
      v4u v[8];
      const v4u lv = mb_ld(rs, lo);
#pragma unroll
      for (int k = 0; k < 4; ++k) { v[2 * k] = mb_ld(rs, oo + k * 512); v[2 * k + 1] = mb_ld(rs, oo + k * 512 + 128); }
      const float lm = fmaxf(fmaxf(uf(lv[0]), uf(lv[1])), fmaxf(uf(lv[2]), uf(lv[3])));
      float e0 = __builtin_amdgcn_exp2f(uf(lv[0]) - lm), e1 = __builtin_amdgcn_exp2f(uf(lv[1]) - lm), e2 = __builtin_amdgcn_exp2f(uf(lv[2]) - lm), e3 = __builtin_amdgcn_exp2f(uf(lv[3]) - lm);
      const float inv = __builtin_amdgcn_rcpf((e0 + e1) + (e2 + e3));
      e0 *= inv; e1 *= inv; e2 *= inv; e3 *= inv;
      oa = (__builtin_bit_cast(v4f, v[0]) * e0 + __builtin_bit_cast(v4f, v[2]) * e1) + (__builtin_bit_cast(v4f, v[4]) * e2 + __builtin_bit_cast(v4f, v[6]) * e3);
      obv = (__builtin_bit_cast(v4f, v[1]) * e0 + __builtin_bit_cast(v4f, v[3]) * e1) + (__builtin_bit_cast(v4f, v[5]) * e2 + __builtin_bit_cast(v4f, v[7]) * e3);
      const v4f ck = oa + obv;
      const float c1n = (ck[0] + ck[1]) + (ck[2] + ck[3]);
      if (__all(c1n == c1n)) break;
      unsigned m = umax4(lv);
#pragma unroll
      for (int k = 0; k < 8; ++k) m = max(m, umax4(v[k]));
      if (__all(m != SENT)) break;
    }
    CNT_KEEP(c0, c1);
    STAMP(6);
    const v4u af = {pk2(oa[0], oa[1]), pk2(oa[2], oa[3]), pk2(obv[0], obv[1]), pk2(obv[2], obv[3])};
#pragma unroll
    for (int e = 0; e < 8; ++e) acc[e] = mfma(af, bt[e], v4f{0.f, 0.f, 0.f, 0.f});
    v4u o0, o1; wtile_out(acc, lane, o0, o1);
    mb_st(rs, off_sp, o0); mb_st(rs, off_sp + 64 * 4096, o1);
    if (lane < 8) cnt_add(ws, WS_C3 + ((m8 * 8 + lane) * 12 + (g & 3)) * 128);
    lds_arrive(lds, SW_WODONE, lane);
    STAMP(7);
  }
  STAMP_FLUSH();
}

extern "C" void kernel_launch(void* const* d_in, const int* in_sizes, int n_in, void* d_out, int out_size, void* d_ws, size_t ws_size, hipStream_t stream) {
  Params p;
  p.x = (const u16*)d_in[0]; p.kc = (const u16*)d_in[1]; p.vc = (const u16*)d_in[2]; p.wq = (const u16*)d_in[3]; p.wo = (const u16*)d_in[4];
  p.wg = (const u16*)d_in[6]; p.wu = (const u16*)d_in[7]; p.wd = (const u16*)d_in[8];
  p.out = (u16*)d_out; p.ws = (unsigned char*)d_ws;
  (void)hipFuncSetAttribute((const void*)fused_decode, hipFuncAttributeMaxDynamicSharedMemorySize, LDS_BYTES);
  fused_decode<<<NWG, NTHR, LDS_BYTES, stream>>>(p);
}
```

```cpp
#include <hip/hip_runtime.h>
#include <hip/hip_bf16.h>
#include <stdint.h>

#define STAMPS 0
#define TWIN 4
#define C2REP 16
#define Q_DELAY 16
#define G_DELAY 24
#define WO_LATE 2
#define GO_AT 0
#define F_DELAY 0
#define C3SH 1
#define H_AT G_VISSUED
#define H_CNT 4u
#define WO_TIME 750
#define KV_IL 2
#define SWZ 1
#define K2 2u
#define K3 4u
#define WO_CNT 0u
#define WO_ARR 2u
#define WD_LATE 0
#define EARLYPOLL 1
#define D2 5
#define D3 5
#define F_LATE 0
#define PFN 4
#define CODE_LINES 112

typedef unsigned short u16;
typedef unsigned long long u64;
typedef unsigned v4u __attribute__((ext_vector_type(4)));
typedef unsigned v2u __attribute__((ext_vector_type(2)));
typedef float v4f __attribute__((ext_vector_type(4)));
typedef __bf16 bf16x8 __attribute__((ext_vector_type(8)));
typedef __bf16 bf16x2 __attribute__((ext_vector_type(2)));
typedef float v2f __attribute__((ext_vector_type(2)));

constexpr int DM = 512, HQ = 32, HKV = 8, SK = 4096, DH = 128, WIN = 1024, FF = 1792;
constexpr int NWG = 256, NWAVE = 12, NTHR = NWAVE * 64;
constexpr int W_C = 0, W_Q = 1, W_F = 2, W_O = 3, W_K0 = 4, W_V0 = 8;

constexpr unsigned WS_EPOCH = 0;
constexpr unsigned WS_LSE = 4096;
constexpr unsigned WS_C2 = WS_LSE + 16384;
constexpr unsigned WS_C3 = WS_C2 + 256 * 128;
constexpr unsigned WS_QP = WS_C3 + 512 * 128;
constexpr unsigned WS_GU = WS_QP + 256 * 4096;
constexpr unsigned WS_OP = WS_GU + 224 * 4096;
constexpr unsigned WS_HB = WS_OP + 256 * 2560;
constexpr unsigned WS_SP = WS_HB + 32 * 2048;
constexpr unsigned WS_DP = WS_SP + 256 * 4096;
constexpr unsigned WS_END = WS_DP + 256 * 4096;
constexpr unsigned WS_STAMP = 8u << 20;

constexpr u64 EPOCH_STEP = 0x9E3779B97F4A7C15ull;
constexpr unsigned SPIN_CAP = 1u << 18;
constexpr unsigned HINT_CAP = 128;
constexpr int AUX_SC1 = 16, AUX_NT = 2;

constexpr int L_W = 0;
constexpr int L_G = 128;
constexpr int L_T = 256;
constexpr int L_MX = 272;
constexpr int L_LS = L_MX + 64;
constexpr int L_Q = L_LS + 64;
constexpr int L_U = L_Q + 1024;
constexpr int L_P = L_U + 2048;
constexpr int L_O = L_P + 8192;
constexpr int XN_STRIDE = 1040;
constexpr int L_XN = L_O + 4 * 2048;
constexpr int L_GATE = L_XN + 8 * XN_STRIDE;
constexpr int L_END = L_GATE + 256;
constexpr int LDS_BYTES = 96 * 1024;
static_assert(L_Q % 16 == 0 && L_END <= LDS_BYTES, "lds");
enum { SW_QISSUED, SW_TREADY, SW_QPUB, SW_GO, SW_FISSUED, SW_KISSUED, SW_VISSUED, SW_QREADY, SW_MAXES, SW_PREADY, SW_ARRIVE, SW_ATTDONE, SW_WODONE, SW_FPUB, SW_UREADY, SW_XN };

__device__ __forceinline__ float bflo(unsigned w) { return __uint_as_float(w << 16); }
__device__ __forceinline__ float bfhi(unsigned w) { return __uint_as_float(w & 0xffff0000u); }
__device__ __forceinline__ unsigned pk2(float lo, float hi) {
  v2f f = {lo, hi};
  bf16x2 b = __builtin_convertvector(f, bf16x2);
  return __builtin_bit_cast(unsigned, b);
}
__device__ __forceinline__ float rbf(float x) { return __uint_as_float(pk2(x, 0.f) << 16); }
__device__ __forceinline__ v4f mfma(v4u a, v4u b, v4f c) {
  return __builtin_amdgcn_mfma_f32_16x16x32_bf16(__builtin_bit_cast(bf16x8, a), __builtin_bit_cast(bf16x8, b), c, 0, 0, 0);
}
template <int CTRL> __device__ __forceinline__ float dpp(float x) {
  return __builtin_bit_cast(float, __builtin_amdgcn_mov_dpp(__builtin_bit_cast(int, x), CTRL, 0xf, 0xf, true));
}
constexpr int XOR1 = 0xB1, ROR8 = 0x128, ROR4 = 0x124;
__device__ __forceinline__ float swap16_add(float a, float b) {
  auto s = __builtin_amdgcn_permlane16_swap(__float_as_uint(a), __float_as_uint(b), false, false);
  return __uint_as_float(s[0]) + __uint_as_float(s[1]);
}
__device__ __forceinline__ float swap32_add(float a, float b) {
  auto s = __builtin_amdgcn_permlane32_swap(__float_as_uint(a), __float_as_uint(b), false, false);
  return __uint_as_float(s[0]) + __uint_as_float(s[1]);
}
__device__ __forceinline__ float xrow_sum(float x) { x = swap16_add(x, x); return swap32_add(x, x); }
__device__ __forceinline__ float xrow_max(float x) {
  auto s = __builtin_amdgcn_permlane16_swap(__float_as_uint(x), __float_as_uint(x), false, false);
  x = fmaxf(__uint_as_float(s[0]), __uint_as_float(s[1]));
  auto t = __builtin_amdgcn_permlane32_swap(__float_as_uint(x), __float_as_uint(x), false, false);
  return fmaxf(__uint_as_float(t[0]), __uint_as_float(t[1]));
}
#define BARRIER() do { asm volatile("s_waitcnt lgkmcnt(0)" ::: "memory"); __builtin_amdgcn_s_barrier(); asm volatile("" ::: "memory"); } while (0)
#define SCHED_FENCE() __builtin_amdgcn_sched_barrier(0)

typedef __amdgpu_buffer_rsrc_t rsrc_t;
__device__ __forceinline__ rsrc_t mkrs(const void* p, unsigned bytes) { return __builtin_amdgcn_make_buffer_rsrc((void*)p, 0, bytes, 0x00020000); }
__device__ __forceinline__ v4u mb_ld(rsrc_t rs, unsigned off) { return __builtin_amdgcn_raw_buffer_load_b128(rs, off, 0, AUX_SC1); }
__device__ __forceinline__ void mb_st(rsrc_t rs, unsigned off, v4u v) { __builtin_amdgcn_raw_buffer_store_b128(v, rs, off, 0, AUX_SC1); }
__device__ __forceinline__ void mb_st1(rsrc_t rs, unsigned off, unsigned v) { __builtin_amdgcn_raw_buffer_store_b32(v, rs, off, 0, AUX_SC1); }
__device__ __forceinline__ float uf(unsigned u) { return __uint_as_float(u); }

__device__ __forceinline__ void tile_tr(v4u (&bt)[8], const v4u (&w)[8]) {
#pragma unroll
  for (int u = 0; u < 4; ++u)
#pragma unroll
    for (int c = 0; c < 4; ++c) {
      bt[2 * c][u] = __builtin_amdgcn_perm(w[2 * u + 1][c], w[2 * u][c], 0x05040100u);
      bt[2 * c + 1][u] = __builtin_amdgcn_perm(w[2 * u + 1][c], w[2 * u][c], 0x07060302u);
    }
}
__device__ __forceinline__ v4u gran(float d0, float d1, v2u T) { return v4u{__float_as_uint(d0), T[0], __float_as_uint(d1), T[1]}; }
__device__ __forceinline__ unsigned gacc(unsigned acc, v4u v, v2u T) {
  acc = __builtin_amdgcn_bitop3_b32(acc, v[1], T[0], 0xF6);
  return __builtin_amdgcn_bitop3_b32(acc, v[3], T[1], 0xF6);
}
template <int N> __device__ __forceinline__ bool gall(const v4u (&v)[N], v2u T, unsigned acc = 0u) {
#pragma unroll
  for (int k = 0; k < N; ++k) acc = gacc(acc, v[k], T);
  return __all(acc == 0u);
}
__device__ __forceinline__ void wtile_out(const v4f (&acc)[8], int lane, float (&o)[8]) {
  const bool ks = (lane & 8) != 0;
#pragma unroll
  for (int e = 0; e < 8; ++e) {
    float a0 = acc[e][0], a1 = acc[e][1], a2 = acc[e][2], a3 = acc[e][3];
    asm("" : "+v"(a1), "+v"(a3));
    const float send = ks ? a1 : a2;
    const float keep = ks ? a3 : a0;
    o[e] = keep + dpp<ROR8>(send);
  }
}
__device__ __forceinline__ void sum4(const v4u (&v)[16], float (&o)[8]) {
#pragma unroll
  for (int pr = 0; pr < 4; ++pr) {
    o[2 * pr] = (uf(v[pr][0]) + uf(v[4 + pr][0])) + (uf(v[8 + pr][0]) + uf(v[12 + pr][0]));
    o[2 * pr + 1] = (uf(v[pr][2]) + uf(v[4 + pr][2])) + (uf(v[8 + pr][2]) + uf(v[12 + pr][2]));
  }
}

typedef __attribute__((address_space(3))) unsigned lds_u32;
__device__ __forceinline__ void lds_set(unsigned char* lds, int word) {
  asm volatile("s_waitcnt lgkmcnt(0)" ::: "memory");
  __hip_atomic_store((lds_u32*)(lds + L_W) + word, 1u, __ATOMIC_RELAXED, __HIP_MEMORY_SCOPE_WORKGROUP);
}
__device__ __forceinline__ void lds_wait(unsigned char* lds, int word, unsigned want) {
#pragma nounroll
  for (unsigned spin = 0; spin < (SPIN_CAP << 4); ++spin) {
    const unsigned v = __hip_atomic_load((lds_u32*)(lds + L_W) + word, __ATOMIC_RELAXED, __HIP_MEMORY_SCOPE_WORKGROUP);
    if (__builtin_amdgcn_readfirstlane(v) >= want) break;
    __builtin_amdgcn_s_sleep(1);
  }
  asm volatile("" ::: "memory");
}
__device__ __forceinline__ unsigned lds_peek(unsigned char* lds, int word) {
  return __builtin_amdgcn_readfirstlane(__hip_atomic_load((lds_u32*)(lds + L_W) + word, __ATOMIC_RELAXED, __HIP_MEMORY_SCOPE_WORKGROUP));
}
__device__ __forceinline__ void lds_spin(unsigned char* lds, int word, unsigned want) {
#pragma nounroll
  for (unsigned spin = 0; spin < (SPIN_CAP << 5); ++spin) { if (lds_peek(lds, word) >= want) break; }
  asm volatile("" ::: "memory");
}
enum { G_KISSUED, G_VISSUED, G_MAXES, G_PREADY, G_ARRIVE, G_XN0, G_XN1, G_U };
typedef __attribute__((address_space(3))) v4u lds_v4u;
__device__ __forceinline__ void grp_set(unsigned char* lds, int grp, int i) {
  asm volatile("s_waitcnt lgkmcnt(0)" ::: "memory");
  __hip_atomic_store((lds_u32*)(lds + L_G + grp * 16) + i, 1u, __ATOMIC_RELAXED, __HIP_MEMORY_SCOPE_WORKGROUP);
}
__device__ __forceinline__ unsigned grp_count(unsigned char* lds, int grp) {
  const v4u f = *(volatile lds_v4u*)(lds + L_G + grp * 16);
  return __builtin_amdgcn_readfirstlane((f[0] + f[1]) + (f[2] + f[3]));
}
template <int SLEEP> __device__ __forceinline__ void grp_wait(unsigned char* lds, int grp, unsigned want) {
#pragma nounroll
  for (unsigned spin = 0; spin < (SPIN_CAP << 5); ++spin) { if (grp_count(lds, grp) >= want) break; if (SLEEP) __builtin_amdgcn_s_sleep(SLEEP); }
  asm volatile("" ::: "memory");
}
__device__ __forceinline__ unsigned lds_arrive(unsigned char* lds, int word, int lane) {
  asm volatile("s_waitcnt lgkmcnt(0)" ::: "memory");
  unsigned old = 0;
  if (lane == 0) old = __hip_atomic_fetch_add((lds_u32*)(lds + L_W) + word, 1u, __ATOMIC_RELAXED, __HIP_MEMORY_SCOPE_WORKGROUP);
  old = __builtin_amdgcn_readfirstlane(old);
  asm volatile("" ::: "memory");
  return old;
}
constexpr int XOR2 = 0x4E, HMIR = 0x141;
__device__ __forceinline__ float wave_sum(float x) {
  x += dpp<XOR1>(x); x += dpp<XOR2>(x); x += dpp<HMIR>(x); x += dpp<ROR8>(x);
  return xrow_sum(x);
}
__device__ __forceinline__ v4u ln_row(v4u xr) {
  float xv[8];
#pragma unroll
  for (int c = 0; c < 4; ++c) { xv[2 * c] = bflo(xr[c]); xv[2 * c + 1] = bfhi(xr[c]); }
  const float mean = wave_sum(((xv[0] + xv[1]) + (xv[2] + xv[3])) + ((xv[4] + xv[5]) + (xv[6] + xv[7]))) * (1.f / DM);
  float q = 0.f;
#pragma unroll
  for (int c = 0; c < 8; ++c) { const float d = xv[c] - mean; q = fmaf(d, d, q); }
  const float var = wave_sum(q) * (1.f / DM);
  const float mb = rbf(mean);
  const float den = rbf(sqrtf(rbf(rbf(var) + 1e-5f)));
  const float r = 1.f / den;
  v4u o;
#pragma unroll
  for (int k = 0; k < 4; ++k) {
    const unsigned n2 = pk2(xv[2 * k] - mb, xv[2 * k + 1] - mb);
    const float n0 = bflo(n2), n1 = bfhi(n2);
    const float t0 = n0 * r, t1 = n1 * r;
    o[k] = pk2(fmaf(fmaf(-t0, den, n0), r, t0), fmaf(fmaf(-t1, den, n1), r, t1));
  }
  return o;
}
__device__ __forceinline__ void cnt_add(unsigned char* ws, unsigned off) {
  __hip_atomic_fetch_add((unsigned*)(ws + off), 1u, __ATOMIC_RELAXED, __HIP_MEMORY_SCOPE_AGENT);
}
#define CNT_PEEK(r) "v_mov_b32 " r ", 0\n\tbuffer_atomic_add " r ", %2, %3, 0 offen sc0"
__device__ __forceinline__ void cnt_wait(v4u srd, unsigned off, int lane, int n, unsigned want, unsigned& r0, unsigned& r1, unsigned slack = 0u) {
  want -= slack;
  const bool idle = lane >= n;
  off = idle ? 0xfffffff0u : off;
  r1 = 0u;
  asm volatile(CNT_PEEK("%0") : "=&v"(r0), "+v"(r1) : "v"(off), "s"(srd) : "memory");
  __builtin_amdgcn_s_sleep(2);
#pragma nounroll
  for (unsigned spin = 0; spin < HINT_CAP; ++spin) {
    asm volatile(CNT_PEEK("%1") "\n\ts_waitcnt vmcnt(1)" : "+v"(r0), "+v"(r1) : "v"(off), "s"(srd) : "memory");
    if (__all(idle || r0 - want <= slack)) break;
    asm volatile(CNT_PEEK("%0") "\n\ts_waitcnt vmcnt(1)" : "+v"(r0), "+v"(r1) : "v"(off), "s"(srd) : "memory");
    if (__all(idle || r1 - want <= slack)) break;
  }
}
#define CNT_KEEP(r0, r1) asm volatile("" :: "v"(r0), "v"(r1))
__device__ __forceinline__ v4u mksrd(const void* p, unsigned bytes) {
  const u64 a = (u64)p;
  return v4u{(unsigned)a, (unsigned)(a >> 32) & 0xffffu, bytes, 0x00020000u};
}
__device__ __forceinline__ v2u get_tag(unsigned char* lds) {
  lds_wait(lds, SW_TREADY, 1u);
  const v2u t = *(const v2u*)(lds + L_T);
  return v2u{(unsigned)__builtin_amdgcn_readfirstlane(t[0]), (unsigned)__builtin_amdgcn_readfirstlane(t[1])};
}
struct Params {
  const u16* x; const u16* kc; const u16* vc; const u16* wq; const u16* wo; const u16* wg; const u16* wu; const u16* wd;
  u16* out; unsigned char* ws;
};

#define STAMP(i) do {} while (0)
#define STAMP_FLUSH() do {} while (0)

__global__ void __launch_bounds__(NTHR, 3) fused_decode(Params p) {
  extern __shared__ __attribute__((aligned(16))) unsigned char lds[];
  const int tid = threadIdx.x, lane = tid & 63;
  const int wave = __builtin_amdgcn_readfirstlane(tid >> 6);
  STAMP(0);
  u64 pc0; asm volatile("s_getpc_b64 %0" : "=s"(pc0));
  const u64 t_start = __builtin_amdgcn_s_memrealtime();
  if (wave == W_O && (blockIdx.x >> 3) < PFN) {
    unsigned pf0, pf1;
    const unsigned char* cp = (const unsigned char*)(pc0 & ~127ull) + lane * 128;
    asm volatile("global_load_dword %0, %2, off\n\tglobal_load_dword %1, %3, off\n\ts_barrier\n\ts_waitcnt vmcnt(0)" : "=&v"(pf0), "=&v"(pf1) : "v"(cp), "v"(cp + (CODE_LINES - 64) * 128) : "memory");
  } else {
    if (tid < 64) *(unsigned*)(lds + L_W + tid * 4) = 0u;
    BARRIER();
  }
  v4u xr = {0u, 0u, 0u, 0u};
  if (wave >= W_K0) xr = *(const v4u*)(p.x + (wave - W_K0) * DM + lane * 8);
  if (wave == W_C) {
    u64 E; asm volatile("s_load_dwordx2 %0, %1, 0x0\n\ts_waitcnt lgkmcnt(0)" : "=s"(E) : "s"(p.ws) : "memory");
    const u64 T64 = E + EPOCH_STEP;
    if (lane == 0) *(v2u*)(lds + L_T) = v2u{(unsigned)T64, (unsigned)(T64 >> 32)};
    lds_set(lds, SW_TREADY);
    STAMP(1);
  }
  SCHED_FENCE();
  const int w = blockIdx.x, g = w & 7, j = w >> 3;
  const int n16 = lane & 15, x4 = lane >> 4;
  const int ob = 2 * x4 + (n16 >> 3), oco = n16 & 7;
  const int ab = 2 * (n16 >> 2) + ((n16 & 3) >> 1), aks = n16 & 1;
  const int k4 = j >> 3, m8 = j & 7;
  const int f_t = k4 * 8 + g;
  const bool has_f = f_t < 28;
  const int a_b = j >> 2, a_s = j & 3;
  const bool is_q = wave == W_Q, is_f = wave == W_F, is_o = wave == W_O;
  constexpr float LOG2E = 1.4426950408889634f;
  const unsigned wt_row = 32 * (n16 >> 3) + 8 * x4;

  if (wave >= W_K0) {
    {
      const int row = wave - W_K0;
      *(v4u*)(lds + L_XN + row * XN_STRIDE + lane * 16) = ln_row(xr);
      grp_set(lds, G_XN0 + (row >> 2), row & 3);
    }
    unsigned char* ws = p.ws; asm volatile("; role A" : "+s"(ws));
    const rsrc_t rs = mkrs(ws, WS_END);
    const bool is_k = wave < W_V0;
    const int a = (wave - W_K0) & 3;
    const size_t kvb = ((size_t)(a_b * HKV + g) * SK + (SK - WIN) + (8 * a + ((a_s + 2 * a) & 3)) * 32) * (DH * 2);
    const unsigned T2 = (4 + ((a_s + 2 * a + 1) & 3) - ((a_s + 2 * a) & 3)) * 8192;
    const int blk = 2 * a + (is_k ? 0 : 1);
    const unsigned off_op = lane < (is_k ? 17 : 16) ? WS_OP + (g * 32 + j) * 2560 + a * 640 + (is_k ? 0 : 384) + lane * 16 : 0xfffffff0u;
    float lse = 0.f;
    const unsigned char* osrc = lds + L_O + ((lane >> 3) & 1) * 1024 + (blk * 8 + (lane & 7)) * 16;
    float inv;
    v2u T;
    if (is_k) {
      v4u r[16];
      {
        const rsrc_t rs1 = mkrs((const unsigned char*)p.kc + kvb, T2 + 8192);
        const unsigned vo = (n16 >> 2) * 256 + x4 * 64 + (n16 & 3) * 16;
        SCHED_FENCE(); lds_wait(lds, SW_FISSUED, 1u); SCHED_FENCE();
#pragma unroll
        for (int t = 0; t < 16; ++t) {
          if (t == TWIN) { lds_spin(lds, SW_GO, 1u); SCHED_FENCE(); }
          r[t] = __builtin_amdgcn_raw_buffer_load_b128(rs1, vo + (t & 3) * 1024, ((t >> 2) & 1) * 4096 + (t >> 3) * T2, AUX_NT);
          SCHED_FENCE();
        }
        grp_set(lds, G_KISSUED, a);
      }
      STAMP(1);
      T = get_tag(lds);
      lds_wait(lds, SW_QREADY, 1u);
      STAMP(2);
      const v4u qf = *(const v4u*)(lds + L_Q + lane * 16);
      float sc[16];
#pragma unroll
      for (int t = 0; t < 16; t += 2) {
        const v4f d = mfma(r[t], qf, v4f{0.f, 0.f, 0.f, 0.f});
        const v4f e = mfma(r[t + 1], qf, v4f{0.f, 0.f, 0.f, 0.f});
        float v0 = dpp<0x00>(d[0]); v0 += dpp<0x55>(d[1]); v0 += dpp<0xAA>(d[2]); v0 += dpp<0xFF>(d[3]);
        float v1 = dpp<0x00>(e[0]); v1 += dpp<0x55>(e[1]); v1 += dpp<0xAA>(e[2]); v1 += dpp<0xFF>(e[3]);
        const unsigned u0 = pk2(v0, v1);
        const unsigned u1 = pk2(bflo(u0) * 0.08838834764831845f, bfhi(u0) * 0.08838834764831845f);
        sc[t] = bflo(u1); sc[t + 1] = bfhi(u1);
      }
      float mx = sc[0];
#pragma unroll
      for (int t = 1; t < 16; ++t) mx = fmaxf(mx, sc[t]);
      mx = xrow_max(mx);
      const int hs = n16 >> 2;
      if (lane < 16 && (lane & 3) == 0) *(float*)(lds + L_MX + (hs * 4 + a) * 4) = mx;
      grp_set(lds, G_MAXES, a);
      grp_wait<0>(lds, G_MAXES, 4u);
      { const v4f m0 = *(const v4f*)(lds + L_MX + hs * 16); mx = fmaxf(fmaxf(m0[0], m0[1]), fmaxf(m0[2], m0[3])); }
      const float mxl = mx * LOG2E;
      float pr[16], ls = 0.f;
#pragma unroll
      for (int t = 0; t < 16; ++t) { pr[t] = __builtin_amdgcn_exp2f(fmaf(sc[t], LOG2E, -mxl)); ls += pr[t]; }
      ls = xrow_sum(ls);
      if (lane < 16 && (lane & 3) == 0) *(float*)(lds + L_LS + (hs * 4 + a) * 4) = ls;
      *(v4u*)(lds + L_P + (2 * a) * 1024 + lane * 16) = v4u{pk2(pr[0], pr[1]), pk2(pr[2], pr[3]), pk2(pr[4], pr[5]), pk2(pr[6], pr[7])};
      *(v4u*)(lds + L_P + (2 * a + 1) * 1024 + lane * 16) = v4u{pk2(pr[8], pr[9]), pk2(pr[10], pr[11]), pk2(pr[12], pr[13]), pk2(pr[14], pr[15])};
      grp_set(lds, G_PREADY, a);
      STAMP(3);
      grp_wait<1>(lds, G_PREADY, 4u);
      const v4f l0 = *(const v4f*)(lds + L_LS + a * 16), m0 = *(const v4f*)(lds + L_MX + a * 16);
      const float L = (l0[0] + l0[1]) + (l0[2] + l0[3]);
      inv = __builtin_amdgcn_rcpf(L);
      lse = fmaf(fmaxf(fmaxf(m0[0], m0[1]), fmaxf(m0[2], m0[3])), LOG2E, __builtin_amdgcn_logf(L));
      grp_wait<1>(lds, G_ARRIVE, 3u);
    } else {
      v4u r[16];
      {
        const rsrc_t rs2 = mkrs((const unsigned char*)p.vc + kvb, T2 + 8192);
        SCHED_FENCE(); grp_wait<1>(lds, G_KISSUED, 4u); SCHED_FENCE();
#pragma unroll
        for (int t = 0; t < 16; ++t) r[t] = __builtin_amdgcn_raw_buffer_load_b128(rs2, lane * 16 + (t & 3) * 1024, ((t >> 2) & 1) * 4096 + (t >> 3) * T2, AUX_NT);
        SCHED_FENCE();
        grp_set(lds, G_VISSUED, a);
      }
      STAMP(1);
      T = get_tag(lds);
      grp_wait<1>(lds, G_PREADY, 4u);
      STAMP(2);
      const v4u p0 = *(const v4u*)(lds + L_P + (2 * a) * 1024 + lane * 16), p1 = *(const v4u*)(lds + L_P + (2 * a + 1) * 1024 + lane * 16);
      { const v4f l0 = *(const v4f*)(lds + L_LS + a * 16); inv = __builtin_amdgcn_rcpf((l0[0] + l0[1]) + (l0[2] + l0[3])); }
      asm volatile("" : "+v"(inv));
      v4f acc[8];
#pragma unroll
      for (int h = 0; h < 2; ++h) {
        v4u bt[8];
#pragma unroll
        for (int u = 0; u < 4; ++u) {
          SCHED_FENCE();
#pragma unroll
          for (int c = 0; c < 4; ++c) {
            bt[2 * c][u] = __builtin_amdgcn_perm(r[8 * h + 2 * u + 1][c], r[8 * h + 2 * u][c], 0x05040100u);
            bt[2 * c + 1][u] = __builtin_amdgcn_perm(r[8 * h + 2 * u + 1][c], r[8 * h + 2 * u][c], 0x07060302u);
          }
        }
#pragma unroll
        for (int e = 0; e < 8; ++e) acc[e] = mfma(h ? p1 : p0, bt[e], h ? acc[e] : v4f{0.f, 0.f, 0.f, 0.f});
      }
      *(v4f*)(lds + L_O + a * 2048 + lane * 16) = v4f{acc[0][0], acc[1][0], acc[2][0], acc[3][0]};
      *(v4f*)(lds + L_O + a * 2048 + 1024 + lane * 16) = v4f{acc[4][0], acc[5][0], acc[6][0], acc[7][0]};
      grp_set(lds, G_ARRIVE, a);
      STAMP(3);
    }
    grp_wait<0>(lds, G_ARRIVE, 4u);
    STAMP(4);
    {
      const v4f o0 = *(const v4f*)(osrc), o1 = *(const v4f*)(osrc + 2048), o2 = *(const v4f*)(osrc + 4096), o3 = *(const v4f*)(osrc + 6144);
      const v4f o = ((o0 + o1) + (o2 + o3)) * inv;
      mb_st(rs, off_op, v4u{lane == 16 ? __float_as_uint(lse) : pk2(o[0], o[1]), T[0], pk2(o[2], o[3]), T[1]});
      if (wave == W_K0) {
        if (lane < C2REP) cnt_add(ws, WS_C2 + (g * 32 + lane) * 128);
        lds_set(lds, SW_ATTDONE);
      }
    }
    STAMP(5);
    STAMP_FLUSH();
    return;
  }

  if (wave == W_C) {
    unsigned char* ws = p.ws; asm volatile("; role C" : "+s"(ws));
    const rsrc_t rs = mkrs(ws, WS_END);
    const v4u srd = mksrd(ws, WS_END);
    const v2u T = get_tag(lds);
    const unsigned co = WS_C3 + ((m8 * 8 + g) * 8) * 128;
    {
      const unsigned so = lane == 0 ? (j < C2REP ? WS_C2 + (g * 32 + j) * 128 : 0xfffffff0u) : (lane < 6 && k4 == 0) ? co + (lane - 1) * 128 : 0xfffffff0u;
      mb_st1(rs, so, T[0]);
    }
    const int fidx = g * DM + 64 * m8 + 8 * (lane & 7) + 2 * k4 + (x4 & 1);
    lds_wait(lds, SW_QPUB, 1u);
    __builtin_amdgcn_s_sleep(Q_DELAY);
    STAMP(3);
    {
      const int qcb = 2 * (n16 >> 2) + (x4 >> 1);
      const unsigned qo = WS_QP + (g * 32 + qcb) * 4096 + ((a_b + SWZ * qcb) & 7) * 128 + (4 * (x4 & 1) + (n16 & 3)) * 16;
      v4u v[16];
#pragma nounroll
      for (unsigned spin = 0; spin < SPIN_CAP; ++spin) {
        asm volatile("" ::: "memory");
#pragma unroll
        for (int k = 0; k < 16; ++k) v[k] = __builtin_amdgcn_raw_buffer_load_b128(rs, qo + (k & 3) * 1024, (k >> 2) * 32768, AUX_SC1);
        SCHED_FENCE(); lds_set(lds, SW_GO); SCHED_FENCE();
        if (gall(v, T)) break;
      }
      float q[8];
      sum4(v, q);
      *(v4u*)(lds + L_Q + lane * 16) = v4u{pk2(q[0], q[1]), pk2(q[2], q[3]), pk2(q[4], q[5]), pk2(q[6], q[7])};
    }
    lds_set(lds, SW_QREADY);
    STAMP(4);
    const float xres = bflo(p.x[fidx]);
    {
      unsigned c0, c1;
      lds_wait(lds, SW_ATTDONE, 1u);
      __builtin_amdgcn_s_sleep(24);
      cnt_wait(srd, co + lane * 128, lane, C3SH, T[0] + 32u / C3SH, c0, c1, K3);
      if (lds_peek(lds, SW_WODONE) == 0u) { lds_spin(lds, SW_WODONE, 1u); __builtin_amdgcn_s_sleep(D3); }
      STAMP(6);
      const unsigned so = WS_SP + w * 4096 + lane * 16;
      v4u s[4];
#pragma nounroll
      for (unsigned spin = 0; spin < SPIN_CAP; ++spin) {
        asm volatile("" ::: "memory");
#pragma unroll
        for (int k = 0; k < 4; ++k) s[k] = mb_ld(rs, so + k * 1024);
        if (gall(s, T)) break;
      }
      CNT_KEEP(c0, c1);
      const float v0 = (uf(s[0][0]) + uf(s[1][0])) + (uf(s[2][0]) + uf(s[3][0]));
      const float v1 = (uf(s[0][2]) + uf(s[1][2])) + (uf(s[2][2]) + uf(s[3][2]));
      float t = swap16_add(v0, v1);
      t = swap32_add(t, t);
      t += dpp<ROR8>(t);
      lds_spin(lds, SW_XN, 1u);
      const float gate = *(const float*)(lds + L_GATE + lane * 4);
      const float r = rbf(rbf(t) * gate);
      if (lane < 32 && n16 < 8) p.out[fidx] = (u16)pk2(xres + r, 0.f);
      if (w == 0 && lane == 0) __builtin_amdgcn_raw_buffer_store_b64(T, rs, WS_EPOCH, 0, AUX_SC1);
    }
    STAMP(7);
    STAMP_FLUSH();
    return;
  }

  unsigned char* ws = p.ws; asm volatile("; role QFO" : "+s"(ws));
  const rsrc_t rs = mkrs(ws, WS_END);
  v2u T;
  if (is_o) {
    T = get_tag(lds);
    STAMP(2);
  } else {
    v4u r1[8], r2[8];
    const int kq = is_q ? k4 : (m8 & 3);
    {
      const unsigned stride = is_q ? HQ * DH * 2 : FF * 2;
      const unsigned char* b1 = (is_q ? (const unsigned char*)(p.wq + 512 * g + 64 * m8) : (const unsigned char*)((m8 >> 2 ? p.wu : p.wg) + 64 * f_t)) + (size_t)(128 * kq) * stride;
      const rsrc_t rs1 = mkrs(b1, (is_q || has_f) ? 127 * stride + 128 : 0u);
      const unsigned vo1 = wt_row * stride + oco * 16;
      SCHED_FENCE();
      if (is_f) { lds_wait(lds, SW_QISSUED, 1u); __builtin_amdgcn_s_sleep(F_DELAY); }
      SCHED_FENCE();
#pragma unroll
      for (int t = 0; t < 8; ++t) r1[t] = __builtin_amdgcn_raw_buffer_load_b128(rs1, vo1, t * stride, AUX_NT);
      SCHED_FENCE();
#pragma unroll
      for (int t = 0; t < 8; ++t) r2[t] = __builtin_amdgcn_raw_buffer_load_b128(rs1, vo1, (64 + t) * stride, AUX_NT);
      SCHED_FENCE();
      lds_set(lds, is_q ? SW_QISSUED : SW_FISSUED);
      SCHED_FENCE();
    }
    STAMP(1);
    v4f acc[8];
    float o[8];
    v4u bt[8];
    if (is_q || has_f) {
      T = get_tag(lds);
      grp_wait<0>(lds, G_XN0, 4u); grp_wait<0>(lds, G_XN1, 4u);
      const unsigned char* xa = lds + L_XN + ab * XN_STRIDE + (128 * kq + 32 * aks + 8 * x4) * 2;
      const v4u a0 = *(const v4u*)xa, a1 = *(const v4u*)(xa + 128);
      STAMP(2);
      tile_tr(bt, r1);
#pragma unroll
      for (int e = 0; e < 8; ++e) acc[e] = mfma(a0, bt[e], v4f{0.f, 0.f, 0.f, 0.f});
      tile_tr(bt, r2);
#pragma unroll
      for (int e = 0; e < 8; ++e) acc[e] = mfma(a1, bt[e], acc[e]);
      wtile_out(acc, lane, o);
      const unsigned off_a = (is_q ? WS_QP + (g * 32 + j) * 4096 : WS_GU + (f_t * 8 + m8) * 4096) + ((ob + SWZ * m8) & 7) * 128 + oco * 16;
#pragma unroll
      for (int pr = 0; pr < 4; ++pr) mb_st(rs, off_a + pr * 1024, gran(o[2 * pr], o[2 * pr + 1], T));
      lds_set(lds, is_q ? SW_QPUB : SW_FPUB);
    }
    STAMP(3);
    if (is_f) {
      if (has_f) {
        __builtin_amdgcn_s_sleep(G_DELAY);
        {
          const unsigned go = WS_GU + (f_t * 8 + (lane >> 3)) * 4096 + ((m8 + SWZ * (lane >> 3)) & 7) * 128 + (lane & 7) * 16;
          v4u v[4];
#pragma nounroll
          for (unsigned spin = 0; spin < SPIN_CAP; ++spin) {
            asm volatile("" ::: "memory");
#pragma unroll
            for (int k = 0; k < 4; ++k) v[k] = mb_ld(rs, go + k * 1024);
            if (gall(v, T)) break;
            __builtin_amdgcn_s_sleep(8);
          }
          unsigned hp[4];
#pragma unroll
          for (int k = 0; k < 4; ++k) {
            float a0 = uf(v[k][0]), a1 = uf(v[k][2]);
            a0 += dpp<ROR8>(a0); a1 += dpp<ROR8>(a1);
            a0 = swap16_add(a0, a0); a1 = swap16_add(a1, a1);
            const unsigned r2_ = pk2(a0, a1);
            auto sw = __builtin_amdgcn_permlane32_swap(r2_, r2_, false, false);
            hp[k] = pk2(bflo(sw[0]) * bflo(sw[1]), bfhi(sw[0]) * bfhi(sw[1]));
          }
          const bool hi = (lane & 8) != 0;
          mb_st(rs, lane < 16 ? WS_HB + (f_t * 8 + m8) * 256 + lane * 16 : 0xfffffff0u, v4u{hi ? hp[2] : hp[0], T[0], hi ? hp[3] : hp[1], T[1]});
        }
        STAMP(4);
        grp_wait<1>(lds, H_AT, H_CNT);
        {
          const rsrc_t rs3 = mkrs(p.wd + (size_t)(64 * f_t) * DM + 64 * m8, 63 * 1024 + 128);
#pragma unroll
          for (int t = 0; t < 8; ++t) r1[t] = __builtin_amdgcn_raw_buffer_load_b128(rs3, wt_row * 1024 + oco * 16, t * 1024, AUX_NT);
        }
        SCHED_FENCE();
        v4u hf;
        {
          const unsigned ho = WS_HB + (f_t * 8 + ab) * 256 + (4 * aks + x4) * 16;
          v4u h0, h1;
#pragma nounroll
          for (unsigned spin = 0; spin < SPIN_CAP; ++spin) {
            asm volatile("" ::: "memory");
            h0 = mb_ld(rs, ho); h1 = mb_ld(rs, ho + 128);
            if (__all(gacc(gacc(0u, h0, T), h1, T) == 0u)) break;
            __builtin_amdgcn_s_sleep(8);
          }
          hf = v4u{h0[0], h0[2], h1[0], h1[2]};
        }
        tile_tr(bt, r1);
#pragma unroll
        for (int e = 0; e < 8; ++e) acc[e] = mfma(hf, bt[e], v4f{0.f, 0.f, 0.f, 0.f});
        wtile_out(acc, lane, o);
        const unsigned off_dp = WS_DP + (m8 * 8 + ob) * 4096 + f_t * 128 + oco * 16;
#pragma unroll
        for (int pr = 0; pr < 4; ++pr) mb_st(rs, off_dp + pr * (64 * 4096), gran(o[2 * pr], o[2 * pr + 1], T));
        if (lane < 8) cnt_add(ws, WS_C3 + ((m8 * 8 + lane) * 8 + 4) * 128);
        STAMP(5);
      } else {
        T = get_tag(lds);
      }
    grp_wait<1>(lds, G_ARRIVE, 2u);
    float gate;
    {
      unsigned c0, c1;
      cnt_wait(mksrd(ws, WS_END), WS_C3 + ((m8 * 8 + g) * 8 + 4) * 128, lane, 1, T[0] + 28u, c0, c1);
      const unsigned dof = WS_DP + w * 4096 + lane * 16;
      const bool d3live = lane < 32;
      v4u d[4];
#pragma nounroll
      for (unsigned spin = 0; spin < SPIN_CAP; ++spin) {
        asm volatile("" ::: "memory");
#pragma unroll
        for (int k = 0; k < 3; ++k) d[k] = mb_ld(rs, dof + k * 1024);
        d[3] = mb_ld(rs, d3live ? dof + 3072 : dof);
        if (gall(d, T)) break;
      }
      CNT_KEEP(c0, c1);
      const float v0 = (uf(d[0][0]) + uf(d[1][0])) + (uf(d[2][0]) + (d3live ? uf(d[3][0]) : 0.f));
      const float v1 = (uf(d[0][2]) + uf(d[1][2])) + (uf(d[2][2]) + (d3live ? uf(d[3][2]) : 0.f));
      float t = swap16_add(v0, v1);
      t = swap32_add(t, t);
      t += dpp<ROR8>(t);
      t = rbf(t);
      gate = rbf(t / (1.f + __expf(-t)));
    }
    *(float*)(lds + L_GATE + lane * 4) = gate;
    lds_set(lds, SW_XN);
    STAMP(6);
      STAMP_FLUSH();
      return;
    }
  }
  {
    const int tt = is_o;
    v4u bt[8], r3[8]; v4f acc[8];
    grp_wait<1>(lds, G_VISSUED, 4u);
#pragma nounroll
    for (unsigned spin = 0; spin < SPIN_CAP; ++spin) {
      if (grp_count(lds, G_ARRIVE) >= WO_LATE || __builtin_amdgcn_s_memrealtime() - t_start >= WO_TIME) break;
      __builtin_amdgcn_s_sleep(1);
    }
    {
      const rsrc_t rs3 = mkrs(p.wo + (size_t)((4 * g + k4) * 128 + 64 * tt) * DM + 64 * m8, 63 * 1024 + 128);
#pragma unroll
      for (int t = 0; t < 8; ++t) r3[t] = __builtin_amdgcn_raw_buffer_load_b128(rs3, wt_row * 1024 + oco * 16, t * 1024, AUX_NT);
    }
    const unsigned lo = WS_OP + (g * 32 + (lane >> 3) * 4) * 2560 + k4 * 640 + 256;
    const unsigned oo = WS_OP + (g * 32 + (lane >> 3) * 4) * 2560 + k4 * 640 + tt * 384 + (lane & 7) * 16;
    const int asrc = (ab * 8 + 4 * aks + x4) * 4;
    const unsigned off_sp = WS_SP + (m8 * 8 + ob) * 4096 + (4 * g + k4) * 128 + oco * 16;
    SCHED_FENCE();
    STAMP(5);
    unsigned c0, c1;
    cnt_wait(mksrd(ws, WS_END), WS_C2 + (g * 32 + (j & (C2REP - 1))) * 128, lane, 1, T[0] + 32u, c0, c1, K2);
    if (lds_peek(lds, SW_ATTDONE) == 0u) { lds_spin(lds, SW_ATTDONE, 1u); __builtin_amdgcn_s_sleep(D2); }
    STAMP(4);
    v4u v[8], l[4];
#pragma unroll
    for (int k = 0; k < 4; ++k) l[k] = __builtin_amdgcn_raw_buffer_load_b128(rs, lo, k * 2560, AUX_SC1);
#pragma unroll
    for (int k = 0; k < 8; ++k) v[k] = __builtin_amdgcn_raw_buffer_load_b128(rs, oo + (k & 1) * 128, (k >> 1) * 2560, AUX_SC1);
    SCHED_FENCE();
    tile_tr(bt, r3);
#pragma unroll
    for (int e = 0; e < 8; ++e) asm volatile("" : "+v"(bt[e]));
    SCHED_FENCE();
#pragma nounroll
    for (unsigned spin = 0; spin < SPIN_CAP; ++spin) {
      if (gall(v, T, gacc(gacc(gacc(gacc(0u, l[0], T), l[1], T), l[2], T), l[3], T))) break;
      asm volatile("" ::: "memory");
#pragma unroll
      for (int k = 0; k < 4; ++k) l[k] = __builtin_amdgcn_raw_buffer_load_b128(rs, lo, k * 2560, AUX_SC1);
#pragma unroll
      for (int k = 0; k < 8; ++k) v[k] = __builtin_amdgcn_raw_buffer_load_b128(rs, oo + (k & 1) * 128, (k >> 1) * 2560, AUX_SC1);
    }
    CNT_KEEP(c0, c1);
    STAMP(6);
    const float lm = fmaxf(fmaxf(uf(l[0][0]), uf(l[1][0])), fmaxf(uf(l[2][0]), uf(l[3][0])));
    float e0 = __builtin_amdgcn_exp2f(uf(l[0][0]) - lm), e1 = __builtin_amdgcn_exp2f(uf(l[1][0]) - lm), e2 = __builtin_amdgcn_exp2f(uf(l[2][0]) - lm), e3 = __builtin_amdgcn_exp2f(uf(l[3][0]) - lm);
    { const float inv = __builtin_amdgcn_rcpf((e0 + e1) + (e2 + e3)); e0 *= inv; e1 *= inv; e2 *= inv; e3 *= inv; }
    v4u af;
#pragma unroll
    for (int c = 0; c < 4; ++c) {
      const int hf = c >> 1, wd_ = (c & 1) * 2;
      const unsigned a0 = v[hf][wd_], a1 = v[2 + hf][wd_], a2 = v[4 + hf][wd_], a3 = v[6 + hf][wd_];
      af[c] = __builtin_amdgcn_ds_bpermute(asrc, pk2(fmaf(e0, bflo(a0), e1 * bflo(a1)) + fmaf(e2, bflo(a2), e3 * bflo(a3)), fmaf(e0, bfhi(a0), e1 * bfhi(a1)) + fmaf(e2, bfhi(a2), e3 * bfhi(a3))));
    }
#pragma unroll
    for (int e = 0; e < 8; ++e) acc[e] = mfma(af, bt[e], v4f{0.f, 0.f, 0.f, 0.f});
    float o[8];
    wtile_out(acc, lane, o);
    *(v4f*)(lds + L_U + tt * 1024 + lane * 16) = tt ? v4f{o[0], o[1], o[2], o[3]} : v4f{o[4], o[5], o[6], o[7]};
    grp_set(lds, G_U, tt);
    grp_wait<0>(lds, G_U, 2u);
    {
      const v4f u = *(const v4f*)(lds + L_U + (1 - tt) * 1024 + lane * 16);
      const v4f m = tt ? v4f{o[4], o[5], o[6], o[7]} : v4f{o[0], o[1], o[2], o[3]};
      mb_st(rs, off_sp + (2 * tt) * (64 * 4096), gran(m[0] + u[0], m[1] + u[1], T));
      mb_st(rs, off_sp + (2 * tt + 1) * (64 * 4096), gran(m[2] + u[2], m[3] + u[3], T));
      if (tt == 0) {
        if (lane < 8) cnt_add(ws, WS_C3 + ((m8 * 8 + lane) * 8 + (g & (C3SH - 1))) * 128);
        lds_set(lds, SW_WODONE);
      }
    }
    STAMP(7);
  }
  STAMP_FLUSH();
}

extern "C" void kernel_launch(void* const* d_in, const int* in_sizes, int n_in, void* d_out, int out_size, void* d_ws, size_t ws_size, hipStream_t stream) {
  Params p;
  p.x = (const u16*)d_in[0]; p.kc = (const u16*)d_in[1]; p.vc = (const u16*)d_in[2]; p.wq = (const u16*)d_in[3]; p.wo = (const u16*)d_in[4];
  p.wg = (const u16*)d_in[6]; p.wu = (const u16*)d_in[7]; p.wd = (const u16*)d_in[8];
  p.out = (u16*)d_out;
  p.ws = (unsigned char*)d_ws + ((ws_size - (16u << 20)) & ~(size_t)0xfffff);
  (void)hipFuncSetAttribute((const void*)fused_decode, hipFuncAttributeMaxDynamicSharedMemorySize, LDS_BYTES);
  fused_decode<<<NWG, NTHR, LDS_BYTES, stream>>>(p);
}
```

```cpp
#include <hip/hip_runtime.h>
#include <hip/hip_bf16.h>
#include <stdint.h>

typedef unsigned short u16;
typedef unsigned long long u64;
typedef unsigned v4u __attribute__((ext_vector_type(4)));
typedef unsigned v2u __attribute__((ext_vector_type(2)));
typedef float v4f __attribute__((ext_vector_type(4)));
typedef __bf16 bf16x8 __attribute__((ext_vector_type(8)));
typedef __bf16 bf16x2 __attribute__((ext_vector_type(2)));
typedef float v2f __attribute__((ext_vector_type(2)));

constexpr int DM = 512, HQ = 32, HKV = 8, SK = 4096, DH = 128, WIN = 1024, FF = 1792;
constexpr int NWG = 256, NWAVE = 12, NTHR = NWAVE * 64;
constexpr int W_C = 0, W_Q = 1, W_F = 2, W_O = 3, W_K0 = 4, W_V0 = 8;

constexpr unsigned WS_EPOCH = 0;
constexpr unsigned WS_C2 = 20480;
constexpr unsigned WS_C3 = WS_C2 + 256 * 128;
constexpr unsigned WS_QP = WS_C3 + 512 * 128;
constexpr unsigned WS_GU = WS_QP + 256 * 4096;
constexpr unsigned WS_OP = WS_GU + 224 * 4096;
constexpr unsigned WS_HB = WS_OP + 256 * 2560;
constexpr unsigned WS_SP = WS_HB + 32 * 2048;
constexpr unsigned WS_DP = WS_SP + 256 * 4096;
constexpr unsigned WS_END = WS_DP + 256 * 4096;

constexpr unsigned OOB = 0x7fffff00u;
constexpr u64 EPOCH_STEP = 0x9E3779B97F4A7C15ull;
constexpr unsigned SPIN_CAP = 1u << 18;
constexpr unsigned HINT_CAP = 128;
constexpr int AUX_SC1 = 16, AUX_NT = 2;
constexpr int TWIN = 2, TWIN2 = 6;
constexpr int F_DELAY = 6, Q_DELAY = 16, G_DELAY = 24, D2 = 5, D3 = 5, LSLEEP = 1;
constexpr int C2REP = 8;
constexpr unsigned K2 = 4u, K3 = 8u;
constexpr int PFN = 4, CODE_LINES = 112;

constexpr int L_W = 0;
constexpr int L_G = 128;
constexpr int L_T = 256;
constexpr int L_MX = 272;
constexpr int L_LS = L_MX + 64;
constexpr int L_Q = L_LS + 64;
constexpr int L_U = L_Q + 1024;
constexpr int L_P = L_U + 2048;
constexpr int L_O = L_P + 8192;
constexpr int XN_STRIDE = 1040;
constexpr int L_XN = L_O + 4 * 2048;
constexpr int L_GATE = L_XN + 8 * XN_STRIDE;
constexpr int L_END = L_GATE + 256;
constexpr int LDS_BYTES = 96 * 1024;
static_assert(L_Q % 16 == 0 && L_END <= LDS_BYTES, "lds");
enum { SW_QISSUED, SW_TREADY, SW_QPUB, SW_GO, SW_FISSUED, SW_QREADY, SW_ATTDONE, SW_WODONE, SW_FPUB, SW_GATE };

__device__ __forceinline__ float bflo(unsigned w) { return __uint_as_float(w << 16); }
__device__ __forceinline__ float bfhi(unsigned w) { return __uint_as_float(w & 0xffff0000u); }
__device__ __forceinline__ unsigned pk2(float lo, float hi) {
  v2f f = {lo, hi};
  bf16x2 b = __builtin_convertvector(f, bf16x2);
  return __builtin_bit_cast(unsigned, b);
}
__device__ __forceinline__ float rbf(float x) { return __uint_as_float(pk2(x, 0.f) << 16); }
__device__ __forceinline__ v4f mfma(v4u a, v4u b, v4f c) {
  return __builtin_amdgcn_mfma_f32_16x16x32_bf16(__builtin_bit_cast(bf16x8, a), __builtin_bit_cast(bf16x8, b), c, 0, 0, 0);
}
template <int CTRL> __device__ __forceinline__ float dpp(float x) {
  return __builtin_bit_cast(float, __builtin_amdgcn_mov_dpp(__builtin_bit_cast(int, x), CTRL, 0xf, 0xf, true));
}
constexpr int XOR1 = 0xB1, ROR8 = 0x128;
__device__ __forceinline__ float swap16_add(float a, float b) {
  auto s = __builtin_amdgcn_permlane16_swap(__float_as_uint(a), __float_as_uint(b), false, false);
  return __uint_as_float(s[0]) + __uint_as_float(s[1]);
}
__device__ __forceinline__ float swap32_add(float a, float b) {
  auto s = __builtin_amdgcn_permlane32_swap(__float_as_uint(a), __float_as_uint(b), false, false);
  return __uint_as_float(s[0]) + __uint_as_float(s[1]);
}
__device__ __forceinline__ float xrow_sum(float x) { x = swap16_add(x, x); return swap32_add(x, x); }
__device__ __forceinline__ float xrow_max(float x) {
  auto s = __builtin_amdgcn_permlane16_swap(__float_as_uint(x), __float_as_uint(x), false, false);
  x = fmaxf(__uint_as_float(s[0]), __uint_as_float(s[1]));
  auto t = __builtin_amdgcn_permlane32_swap(__float_as_uint(x), __float_as_uint(x), false, false);
  return fmaxf(__uint_as_float(t[0]), __uint_as_float(t[1]));
}
#define BARRIER() do { asm volatile("s_waitcnt lgkmcnt(0)" ::: "memory"); __builtin_amdgcn_s_barrier(); asm volatile("" ::: "memory"); } while (0)
#define SCHED_FENCE() __builtin_amdgcn_sched_barrier(0)

typedef __amdgpu_buffer_rsrc_t rsrc_t;
__device__ __forceinline__ rsrc_t mkrs(const void* p, unsigned bytes) { return __builtin_amdgcn_make_buffer_rsrc((void*)p, 0, bytes, 0x00020000); }
__device__ __forceinline__ v4u mb_ld(rsrc_t rs, unsigned off) { return __builtin_amdgcn_raw_buffer_load_b128(rs, off, 0, AUX_SC1); }
__device__ __forceinline__ void mb_st(rsrc_t rs, unsigned off, v4u v) { __builtin_amdgcn_raw_buffer_store_b128(v, rs, off, 0, AUX_SC1); }
__device__ __forceinline__ void mb_st1(rsrc_t rs, unsigned off, unsigned v) { __builtin_amdgcn_raw_buffer_store_b32(v, rs, off, 0, AUX_SC1); }
__device__ __forceinline__ float uf(unsigned u) { return __uint_as_float(u); }

__device__ __forceinline__ void tile_tr(v4u (&bt)[8], const v4u (&w)[8]) {
#pragma unroll
  for (int u = 0; u < 4; ++u)
#pragma unroll
    for (int c = 0; c < 4; ++c) {
      bt[2 * c][u] = __builtin_amdgcn_perm(w[2 * u + 1][c], w[2 * u][c], 0x05040100u);
      bt[2 * c + 1][u] = __builtin_amdgcn_perm(w[2 * u + 1][c], w[2 * u][c], 0x07060302u);
    }
}
__device__ __forceinline__ v4u gran(float d0, float d1, v2u T) { return v4u{__float_as_uint(d0), T[0], __float_as_uint(d1), T[1]}; }
__device__ __forceinline__ unsigned gacc(unsigned acc, v4u v, v2u T) {
  acc = __builtin_amdgcn_bitop3_b32(acc, v[1], T[0], 0xF6);
  return __builtin_amdgcn_bitop3_b32(acc, v[3], T[1], 0xF6);
}
template <int N> __device__ __forceinline__ bool gall(const v4u (&v)[N], v2u T, unsigned acc = 0u) {
#pragma unroll
  for (int k = 0; k < N; ++k) acc = gacc(acc, v[k], T);
  return __all(acc == 0u);
}
__device__ __forceinline__ void wtile_out(const v4f (&acc)[8], int lane, float (&o)[8]) {
  const bool ks = (lane & 8) != 0;
#pragma unroll
  for (int e = 0; e < 8; ++e) {
    float a0 = acc[e][0], a1 = acc[e][1], a2 = acc[e][2], a3 = acc[e][3];
    asm("" : "+v"(a1), "+v"(a3));
    const float send = ks ? a1 : a2;
    const float keep = ks ? a3 : a0;
    o[e] = keep + dpp<ROR8>(send);
  }
}
__device__ __forceinline__ void sum4(const v4u (&v)[16], float (&o)[8]) {
#pragma unroll
  for (int pr = 0; pr < 4; ++pr) {
    o[2 * pr] = (uf(v[pr][0]) + uf(v[4 + pr][0])) + (uf(v[8 + pr][0]) + uf(v[12 + pr][0]));
    o[2 * pr + 1] = (uf(v[pr][2]) + uf(v[4 + pr][2])) + (uf(v[8 + pr][2]) + uf(v[12 + pr][2]));
  }
}

typedef __attribute__((address_space(3))) unsigned lds_u32;
__device__ __forceinline__ void lds_set(unsigned char* lds, int word) {
  asm volatile("s_waitcnt lgkmcnt(0)" ::: "memory");
  __hip_atomic_store((lds_u32*)(lds + L_W) + word, 1u, __ATOMIC_RELAXED, __HIP_MEMORY_SCOPE_WORKGROUP);
}
__device__ __forceinline__ void lds_wait(unsigned char* lds, int word, unsigned want) {
#pragma nounroll
  for (unsigned spin = 0; spin < (SPIN_CAP << 4); ++spin) {
    const unsigned v = __hip_atomic_load((lds_u32*)(lds + L_W) + word, __ATOMIC_RELAXED, __HIP_MEMORY_SCOPE_WORKGROUP);
    if (__builtin_amdgcn_readfirstlane(v) >= want) break;
    __builtin_amdgcn_s_sleep(LSLEEP);
  }
  asm volatile("" ::: "memory");
}
__device__ __forceinline__ unsigned lds_peek(unsigned char* lds, int word) {
  return __builtin_amdgcn_readfirstlane(__hip_atomic_load((lds_u32*)(lds + L_W) + word, __ATOMIC_RELAXED, __HIP_MEMORY_SCOPE_WORKGROUP));
}
__device__ __forceinline__ void lds_spin(unsigned char* lds, int word, unsigned want) {
#pragma nounroll
  for (unsigned spin = 0; spin < (SPIN_CAP << 5); ++spin) { if (lds_peek(lds, word) >= want) break; }
  asm volatile("" ::: "memory");
}
enum { G_KISSUED, G_VISSUED, G_MAXES, G_PREADY, G_ARRIVE, G_XN0, G_XN1, G_U };
typedef __attribute__((address_space(3))) v4u lds_v4u;
__device__ __forceinline__ void grp_set(unsigned char* lds, int grp, int i) {
  asm volatile("s_waitcnt lgkmcnt(0)" ::: "memory");
  __hip_atomic_store((lds_u32*)(lds + L_G + grp * 16) + i, 1u, __ATOMIC_RELAXED, __HIP_MEMORY_SCOPE_WORKGROUP);
}
__device__ __forceinline__ unsigned grp_count(unsigned char* lds, int grp) {
  const v4u f = *(volatile lds_v4u*)(lds + L_G + grp * 16);
  return __builtin_amdgcn_readfirstlane((f[0] + f[1]) + (f[2] + f[3]));
}
template <int SLEEP> __device__ __forceinline__ void grp_wait(unsigned char* lds, int grp, unsigned want) {
#pragma nounroll
  for (unsigned spin = 0; spin < (SPIN_CAP << 5); ++spin) { if (grp_count(lds, grp) >= want) break; if (SLEEP) __builtin_amdgcn_s_sleep(SLEEP); }
  asm volatile("" ::: "memory");
}
constexpr int XOR2 = 0x4E, HMIR = 0x141;
__device__ __forceinline__ float wave_sum(float x) {
  x += dpp<XOR1>(x); x += dpp<XOR2>(x); x += dpp<HMIR>(x); x += dpp<ROR8>(x);
  return xrow_sum(x);
}
__device__ __forceinline__ v4u ln_row(v4u xr) {
  float xv[8];
#pragma unroll
  for (int c = 0; c < 4; ++c) { xv[2 * c] = bflo(xr[c]); xv[2 * c + 1] = bfhi(xr[c]); }
  const float mean = wave_sum(((xv[0] + xv[1]) + (xv[2] + xv[3])) + ((xv[4] + xv[5]) + (xv[6] + xv[7]))) * (1.f / DM);
  float q = 0.f;
#pragma unroll
  for (int c = 0; c < 8; ++c) { const float d = xv[c] - mean; q = fmaf(d, d, q); }
  const float var = wave_sum(q) * (1.f / DM);
  const float mb = rbf(mean);
  const float den = rbf(sqrtf(rbf(rbf(var) + 1e-5f)));
  const float r = 1.f / den;
  v4u o;
#pragma unroll
  for (int k = 0; k < 4; ++k) {
    const unsigned n2 = pk2(xv[2 * k] - mb, xv[2 * k + 1] - mb);
    const float n0 = bflo(n2), n1 = bfhi(n2);
    const float t0 = n0 * r, t1 = n1 * r;
    o[k] = pk2(fmaf(fmaf(-t0, den, n0), r, t0), fmaf(fmaf(-t1, den, n1), r, t1));
  }
  return o;
}
__device__ __forceinline__ void cnt_add(unsigned char* ws, unsigned off) {
  __hip_atomic_fetch_add((unsigned*)(ws + off), 1u, __ATOMIC_RELAXED, __HIP_MEMORY_SCOPE_AGENT);
}
#define CNT_PEEK(r) "v_mov_b32 " r ", 0\n\tbuffer_atomic_add " r ", %2, %3, 0 offen sc0"
__device__ __forceinline__ void cnt_wait(v4u srd, unsigned off, int lane, int n, unsigned want, unsigned& r0, unsigned& r1, unsigned slack = 0u) {
  want -= slack;
  const bool idle = lane >= n;
  off = idle ? OOB : off;
  r1 = 0u;
  asm volatile(CNT_PEEK("%0") : "=&v"(r0), "+v"(r1) : "v"(off), "s"(srd) : "memory");
  __builtin_amdgcn_s_sleep(2);
#pragma nounroll
  for (unsigned spin = 0; spin < HINT_CAP; ++spin) {
    asm volatile(CNT_PEEK("%1") "\n\ts_waitcnt vmcnt(1)" : "+v"(r0), "+v"(r1) : "v"(off), "s"(srd) : "memory");
    if (__all(idle || r0 - want <= slack)) break;
    asm volatile(CNT_PEEK("%0") "\n\ts_waitcnt vmcnt(1)" : "+v"(r0), "+v"(r1) : "v"(off), "s"(srd) : "memory");
    if (__all(idle || r1 - want <= slack)) break;
  }
}
#define CNT_KEEP(r0, r1) asm volatile("" :: "v"(r0), "v"(r1))
__device__ __forceinline__ v4u mksrd(const void* p, unsigned bytes) {
  const u64 a = (u64)p;
  return v4u{(unsigned)a, (unsigned)(a >> 32) & 0xffffu, bytes, 0x00020000u};
}
__device__ __forceinline__ v2u get_tag(unsigned char* lds) {
  lds_wait(lds, SW_TREADY, 1u);
  const v2u t = *(const v2u*)(lds + L_T);
  return v2u{(unsigned)__builtin_amdgcn_readfirstlane(t[0]), (unsigned)__builtin_amdgcn_readfirstlane(t[1])};
}
struct Params {
  const u16* x; const u16* kc; const u16* vc; const u16* wq; const u16* wo; const u16* wg; const u16* wu; const u16* wd;
  u16* out; unsigned char* ws;
};


__global__ void __launch_bounds__(NTHR, 3) fused_decode(Params p) {
  extern __shared__ __attribute__((aligned(16))) unsigned char lds[];
  const int tid = threadIdx.x, lane = tid & 63;
  const int wave = __builtin_amdgcn_readfirstlane(tid >> 6);
  u64 pc0; asm volatile("s_getpc_b64 %0" : "=s"(pc0));
  if (wave == W_O && (blockIdx.x >> 3) < PFN) {
    unsigned pf0, pf1;
    const unsigned char* cp = (const unsigned char*)(pc0 & ~127ull) + lane * 128;
    asm volatile("global_load_dword %0, %2, off\n\tglobal_load_dword %1, %3, off\n\ts_barrier\n\ts_waitcnt vmcnt(0)" : "=&v"(pf0), "=&v"(pf1) : "v"(cp), "v"(cp + (CODE_LINES - 64) * 128) : "memory");
  } else {
    if (tid < 64) *(unsigned*)(lds + L_W + tid * 4) = 0u;
    BARRIER();
  }
  v4u xr = {0u, 0u, 0u, 0u};
  if (wave >= W_K0) xr = *(const v4u*)(p.x + (wave - W_K0) * DM + lane * 8);
  if (wave == W_C) {
    u64 E; asm volatile("s_load_dwordx2 %0, %1, 0x0\n\ts_waitcnt lgkmcnt(0)" : "=s"(E) : "s"(p.ws) : "memory");
    const u64 T64 = E + EPOCH_STEP;
    if (lane == 0) *(v2u*)(lds + L_T) = v2u{(unsigned)T64, (unsigned)(T64 >> 32)};
    lds_set(lds, SW_TREADY);
  }
  SCHED_FENCE();
  const int w = blockIdx.x, g = w & 7, j = w >> 3;
  const int n16 = lane & 15, x4 = lane >> 4;
  const int ob = 2 * x4 + (n16 >> 3), oco = n16 & 7;
  const int ab = 2 * (n16 >> 2) + ((n16 & 3) >> 1), aks = n16 & 1;
  const int k4 = j >> 3, m8 = j & 7;
  const int f_t = k4 * 8 + g;
  const bool has_f = f_t < 28;
  const int a_b = j >> 2, a_s = j & 3;
  const bool is_q = wave == W_Q, is_f = wave == W_F, is_o = wave == W_O;
  constexpr float LOG2E = 1.4426950408889634f;
  const unsigned wt_row = 32 * (n16 >> 3) + 8 * x4;

  if (wave >= W_K0) {
    {
      const int row = wave - W_K0;
      *(v4u*)(lds + L_XN + row * XN_STRIDE + lane * 16) = ln_row(xr);
      grp_set(lds, G_XN0 + (row >> 2), row & 3);
    }
    unsigned char* ws = p.ws; asm volatile("; role A" : "+s"(ws));
    const rsrc_t rs = mkrs(ws, WS_END);
    const bool is_k = wave < W_V0;
    const int a = (wave - W_K0) & 3;
    const size_t kvb = ((size_t)(a_b * HKV + g) * SK + (SK - WIN) + (8 * a + ((a_s + 2 * a) & 3)) * 32) * (DH * 2);
    const unsigned T2 = (4 + ((a_s + 2 * a + 1) & 3) - ((a_s + 2 * a) & 3)) * 8192;
    const int blk = 2 * a + (is_k ? 0 : 1);
    const unsigned off_op = lane < (is_k ? 17 : 16) ? WS_OP + (g * 32 + j) * 2560 + a * 640 + (is_k ? 0 : 384) + lane * 16 : OOB;
    float hmax = 0.f, hsum = 0.f;
    const unsigned char* osrc = lds + L_O + ((lane >> 3) & 1) * 1024 + (blk * 8 + (lane & 7)) * 16;
    float inv;
    v2u T;
    if (is_k) {
      v4u r[16];
      {
        const rsrc_t rs1 = mkrs((const unsigned char*)p.kc + kvb, T2 + 8192);
        const unsigned vo = (n16 >> 2) * 256 + x4 * 64 + (n16 & 3) * 16;
        SCHED_FENCE(); lds_wait(lds, SW_FISSUED, 1u); SCHED_FENCE();
#pragma unroll
        for (int t = 0; t < 16; ++t) {
          if (t == TWIN) { lds_spin(lds, SW_QPUB, 1u); SCHED_FENCE(); }
          if (t == TWIN2) { lds_spin(lds, SW_GO, 1u); SCHED_FENCE(); }
          r[t] = __builtin_amdgcn_raw_buffer_load_b128(rs1, vo + (t & 3) * 1024, ((t >> 2) & 1) * 4096 + (t >> 3) * T2, AUX_NT);
          SCHED_FENCE();
        }
        grp_set(lds, G_KISSUED, a);
      }
      T = get_tag(lds);
      lds_wait(lds, SW_QREADY, 1u);
      const v4u qf = *(const v4u*)(lds + L_Q + lane * 16);
      float sc[16];
#pragma unroll
      for (int t = 0; t < 16; t += 2) {
        const v4f d = mfma(r[t], qf, v4f{0.f, 0.f, 0.f, 0.f});
        const v4f e = mfma(r[t + 1], qf, v4f{0.f, 0.f, 0.f, 0.f});
        float v0 = dpp<0x00>(d[0]); v0 += dpp<0x55>(d[1]); v0 += dpp<0xAA>(d[2]); v0 += dpp<0xFF>(d[3]);
        float v1 = dpp<0x00>(e[0]); v1 += dpp<0x55>(e[1]); v1 += dpp<0xAA>(e[2]); v1 += dpp<0xFF>(e[3]);
        const unsigned u0 = pk2(v0, v1);
        const unsigned u1 = pk2(bflo(u0) * 0.08838834764831845f, bfhi(u0) * 0.08838834764831845f);
        sc[t] = bflo(u1); sc[t + 1] = bfhi(u1);
      }
      float mx = sc[0];
#pragma unroll
      for (int t = 1; t < 16; ++t) mx = fmaxf(mx, sc[t]);
      mx = xrow_max(mx);
      const int hs = n16 >> 2;
      if (lane < 16 && (lane & 3) == 0) *(float*)(lds + L_MX + (hs * 4 + a) * 4) = mx;
      grp_set(lds, G_MAXES, a);
      grp_wait<0>(lds, G_MAXES, 4u);
      { const v4f m0 = *(const v4f*)(lds + L_MX + hs * 16); mx = fmaxf(fmaxf(m0[0], m0[1]), fmaxf(m0[2], m0[3])); }
      float pr[16], ls = 0.f;
#pragma unroll
      for (int t = 0; t < 16; ++t) { pr[t] = __builtin_amdgcn_exp2f((sc[t] - mx) * LOG2E); ls += pr[t]; }
      ls = xrow_sum(ls);
      if (lane < 16 && (lane & 3) == 0) *(float*)(lds + L_LS + (hs * 4 + a) * 4) = ls;
      *(v4u*)(lds + L_P + (2 * a) * 1024 + lane * 16) = v4u{pk2(pr[0], pr[1]), pk2(pr[2], pr[3]), pk2(pr[4], pr[5]), pk2(pr[6], pr[7])};
      *(v4u*)(lds + L_P + (2 * a + 1) * 1024 + lane * 16) = v4u{pk2(pr[8], pr[9]), pk2(pr[10], pr[11]), pk2(pr[12], pr[13]), pk2(pr[14], pr[15])};
      grp_set(lds, G_PREADY, a);
      grp_wait<LSLEEP>(lds, G_PREADY, 4u);
      const v4f l0 = *(const v4f*)(lds + L_LS + a * 16), m0 = *(const v4f*)(lds + L_MX + a * 16);
      hsum = (l0[0] + l0[1]) + (l0[2] + l0[3]);
      hmax = fmaxf(fmaxf(m0[0], m0[1]), fmaxf(m0[2], m0[3]));
      inv = __builtin_amdgcn_rcpf(hsum);
      grp_wait<LSLEEP>(lds, G_ARRIVE, 3u);
    } else {
      v4u r[16];
      {
        const rsrc_t rs2 = mkrs((const unsigned char*)p.vc + kvb, T2 + 8192);
        SCHED_FENCE(); grp_wait<LSLEEP>(lds, G_KISSUED, 4u); SCHED_FENCE();
#pragma unroll
        for (int t = 0; t < 16; ++t) r[t] = __builtin_amdgcn_raw_buffer_load_b128(rs2, lane * 16 + (t & 3) * 1024, ((t >> 2) & 1) * 4096 + (t >> 3) * T2, AUX_NT);
        SCHED_FENCE();
        grp_set(lds, G_VISSUED, a);
      }
      T = get_tag(lds);
      grp_wait<LSLEEP>(lds, G_PREADY, 4u);
      const v4u p0 = *(const v4u*)(lds + L_P + (2 * a) * 1024 + lane * 16), p1 = *(const v4u*)(lds + L_P + (2 * a + 1) * 1024 + lane * 16);
      { const v4f l0 = *(const v4f*)(lds + L_LS + a * 16); inv = __builtin_amdgcn_rcpf((l0[0] + l0[1]) + (l0[2] + l0[3])); }
      asm volatile("" : "+v"(inv));
      v4f acc[8];
#pragma unroll
      for (int h = 0; h < 2; ++h) {
        v4u bt[8];
#pragma unroll
        for (int u = 0; u < 4; ++u) {
          SCHED_FENCE();
#pragma unroll
          for (int c = 0; c < 4; ++c) {
            bt[2 * c][u] = __builtin_amdgcn_perm(r[8 * h + 2 * u + 1][c], r[8 * h + 2 * u][c], 0x05040100u);
            bt[2 * c + 1][u] = __builtin_amdgcn_perm(r[8 * h + 2 * u + 1][c], r[8 * h + 2 * u][c], 0x07060302u);
          }
        }
#pragma unroll
        for (int e = 0; e < 8; ++e) acc[e] = mfma(h ? p1 : p0, bt[e], h ? acc[e] : v4f{0.f, 0.f, 0.f, 0.f});
      }
      *(v4f*)(lds + L_O + a * 2048 + lane * 16) = v4f{acc[0][0], acc[1][0], acc[2][0], acc[3][0]};
      *(v4f*)(lds + L_O + a * 2048 + 1024 + lane * 16) = v4f{acc[4][0], acc[5][0], acc[6][0], acc[7][0]};
      grp_set(lds, G_ARRIVE, a);
    }
    grp_wait<0>(lds, G_ARRIVE, 4u);
    {
      const v4f o0 = *(const v4f*)(osrc), o1 = *(const v4f*)(osrc + 2048), o2 = *(const v4f*)(osrc + 4096), o3 = *(const v4f*)(osrc + 6144);
      const v4f o = ((o0 + o1) + (o2 + o3)) * inv;
      mb_st(rs, off_op, v4u{lane == 16 ? __float_as_uint(hmax) : pk2(o[0], o[1]), T[0], lane == 16 ? __float_as_uint(hsum) : pk2(o[2], o[3]), T[1]});
      if (wave == W_K0) {
        if (lane < C2REP) cnt_add(ws, WS_C2 + (g * 32 + lane) * 128);
        lds_set(lds, SW_ATTDONE);
      }
    }
    return;
  }

  if (wave == W_C) {
    unsigned char* ws = p.ws; asm volatile("; role C" : "+s"(ws));
    const rsrc_t rs = mkrs(ws, WS_END);
    const v4u srd = mksrd(ws, WS_END);
    const v2u T = get_tag(lds);
    const unsigned co = WS_C3 + ((m8 * 8 + g) * 8) * 128;
    {
      const unsigned so = lane == 0 ? (j < C2REP ? WS_C2 + (g * 32 + j) * 128 : OOB) : (lane < 6 && k4 == 0) ? co + (lane - 1) * 128 : OOB;
      mb_st1(rs, so, T[0]);
    }
    const int fidx = g * DM + 64 * m8 + 8 * (lane & 7) + 2 * k4 + (x4 & 1);
    lds_wait(lds, SW_QPUB, 1u);
    __builtin_amdgcn_s_sleep(Q_DELAY);
    {
      const int qcb = 2 * (n16 >> 2) + (x4 >> 1);
      const unsigned qo = WS_QP + (g * 32 + qcb) * 4096 + ((a_b + qcb) & 7) * 128 + (4 * (x4 & 1) + (n16 & 3)) * 16;
      v4u v[16];
#pragma nounroll
      for (unsigned spin = 0; spin < SPIN_CAP; ++spin) {
        asm volatile("" ::: "memory");
#pragma unroll
        for (int k = 0; k < 16; ++k) v[k] = __builtin_amdgcn_raw_buffer_load_b128(rs, qo + (k & 3) * 1024, (k >> 2) * 32768, AUX_SC1);
        SCHED_FENCE(); lds_set(lds, SW_GO); SCHED_FENCE();
        if (gall(v, T)) break;
      }
      float q[8];
      sum4(v, q);
      *(v4u*)(lds + L_Q + lane * 16) = v4u{pk2(q[0], q[1]), pk2(q[2], q[3]), pk2(q[4], q[5]), pk2(q[6], q[7])};
    }
    lds_set(lds, SW_QREADY);
    const float xres = bflo(p.x[fidx]);
    {
      unsigned c0, c1;
      lds_wait(lds, SW_ATTDONE, 1u);
      __builtin_amdgcn_s_sleep(24);
      cnt_wait(srd, co, lane, 1, T[0] + 32u, c0, c1, K3);
      if (lds_peek(lds, SW_WODONE) == 0u) { lds_spin(lds, SW_WODONE, 1u); __builtin_amdgcn_s_sleep(D3); }
      const unsigned so = WS_SP + w * 4096 + lane * 16;
      v4u s[4];
#pragma nounroll
      for (unsigned spin = 0; spin < SPIN_CAP; ++spin) {
        asm volatile("" ::: "memory");
#pragma unroll
        for (int k = 0; k < 4; ++k) s[k] = mb_ld(rs, so + k * 1024);
        if (gall(s, T)) break;
      }
      CNT_KEEP(c0, c1);
      const float v0 = (uf(s[0][0]) + uf(s[1][0])) + (uf(s[2][0]) + uf(s[3][0]));
      const float v1 = (uf(s[0][2]) + uf(s[1][2])) + (uf(s[2][2]) + uf(s[3][2]));
      float t = swap16_add(v0, v1);
      t = swap32_add(t, t);
      t += dpp<ROR8>(t);
      lds_spin(lds, SW_GATE, 1u);
      const float gate = *(const float*)(lds + L_GATE + lane * 4);
      const float r = rbf(rbf(t) * gate);
      if (lane < 32 && n16 < 8) p.out[fidx] = (u16)pk2(xres + r, 0.f);
      if (w == 0 && lane == 0) __builtin_amdgcn_raw_buffer_store_b64(T, rs, WS_EPOCH, 0, AUX_SC1);
    }
    return;
  }

  unsigned char* ws = p.ws; asm volatile("; role QFO" : "+s"(ws));
  const rsrc_t rs = mkrs(ws, WS_END);
  v2u T;
  if (is_o) {
    T = get_tag(lds);
  } else {
    v4u r1[8], r2[8];
    const int kq = is_q ? k4 : (m8 & 3);
    {
      const unsigned stride = is_q ? HQ * DH * 2 : FF * 2;
      const unsigned char* b1 = (is_q ? (const unsigned char*)(p.wq + 512 * g + 64 * m8) : (const unsigned char*)((m8 >> 2 ? p.wu : p.wg) + 64 * f_t)) + (size_t)(128 * kq) * stride;
      const rsrc_t rs1 = mkrs(b1, (is_q || has_f) ? 127 * stride + 128 : 0u);
      const unsigned vo1 = wt_row * stride + oco * 16;
      SCHED_FENCE();
      if (is_f) { lds_wait(lds, SW_QISSUED, 1u); __builtin_amdgcn_s_sleep(F_DELAY); }
      SCHED_FENCE();
#pragma unroll
      for (int t = 0; t < 8; ++t) r1[t] = __builtin_amdgcn_raw_buffer_load_b128(rs1, vo1, t * stride, AUX_NT);
      SCHED_FENCE();
#pragma unroll
      for (int t = 0; t < 8; ++t) r2[t] = __builtin_amdgcn_raw_buffer_load_b128(rs1, vo1, (64 + t) * stride, AUX_NT);
      SCHED_FENCE();
      lds_set(lds, is_q ? SW_QISSUED : SW_FISSUED);
      SCHED_FENCE();
    }
    v4f acc[8];
    float o[8];
    v4u bt[8];
    if (is_q || has_f) {
      T = get_tag(lds);
      grp_wait<0>(lds, G_XN0, 4u); grp_wait<0>(lds, G_XN1, 4u);
      const unsigned char* xa = lds + L_XN + ab * XN_STRIDE + (128 * kq + 32 * aks + 8 * x4) * 2;
      const v4u a0 = *(const v4u*)xa, a1 = *(const v4u*)(xa + 128);
      tile_tr(bt, r1);
#pragma unroll
      for (int e = 0; e < 8; ++e) acc[e] = mfma(a0, bt[e], v4f{0.f, 0.f, 0.f, 0.f});
      tile_tr(bt, r2);
#pragma unroll
      for (int e = 0; e < 8; ++e) acc[e] = mfma(a1, bt[e], acc[e]);
      wtile_out(acc, lane, o);
      const unsigned off_a = (is_q ? WS_QP + (g * 32 + j) * 4096 : WS_GU + (f_t * 8 + m8) * 4096) + ((ob + m8) & 7) * 128 + oco * 16;
#pragma unroll
      for (int pr = 0; pr < 4; ++pr) mb_st(rs, off_a + pr * 1024, gran(o[2 * pr], o[2 * pr + 1], T));
      lds_set(lds, is_q ? SW_QPUB : SW_FPUB);
    }
    if (is_f) {
      if (has_f) {
        __builtin_amdgcn_s_sleep(G_DELAY);
        {
          const unsigned go = WS_GU + (f_t * 8 + (lane >> 3)) * 4096 + ((m8 + (lane >> 3)) & 7) * 128 + (lane & 7) * 16;
          v4u v[4];
#pragma nounroll
          for (unsigned spin = 0; spin < SPIN_CAP; ++spin) {
            asm volatile("" ::: "memory");
#pragma unroll
            for (int k = 0; k < 4; ++k) v[k] = mb_ld(rs, go + k * 1024);
            if (gall(v, T)) break;
            __builtin_amdgcn_s_sleep(8);
          }
          unsigned hp[4];
#pragma unroll
          for (int k = 0; k < 4; ++k) {
            float a0 = uf(v[k][0]), a1 = uf(v[k][2]);
            a0 += dpp<ROR8>(a0); a1 += dpp<ROR8>(a1);
            a0 = swap16_add(a0, a0); a1 = swap16_add(a1, a1);
            const unsigned r2_ = pk2(a0, a1);
            auto sw = __builtin_amdgcn_permlane32_swap(r2_, r2_, false, false);
            hp[k] = pk2(bflo(sw[0]) * bflo(sw[1]), bfhi(sw[0]) * bfhi(sw[1]));
          }
          const bool hi = (lane & 8) != 0;
          mb_st(rs, lane < 16 ? WS_HB + (f_t * 8 + m8) * 256 + lane * 16 : OOB, v4u{hi ? hp[2] : hp[0], T[0], hi ? hp[3] : hp[1], T[1]});
        }
        grp_wait<LSLEEP>(lds, G_VISSUED, 4u);
        {
          const rsrc_t rs3 = mkrs(p.wd + (size_t)(64 * f_t) * DM + 64 * m8, 63 * 1024 + 128);
#pragma unroll
          for (int t = 0; t < 8; ++t) r1[t] = __builtin_amdgcn_raw_buffer_load_b128(rs3, wt_row * 1024 + oco * 16, t * 1024, AUX_NT);
        }
        SCHED_FENCE();
        v4u hf;
        {
          const unsigned ho = WS_HB + (f_t * 8 + ab) * 256 + (4 * aks + x4) * 16;
          v4u h0, h1;
#pragma nounroll
          for (unsigned spin = 0; spin < SPIN_CAP; ++spin) {
            asm volatile("" ::: "memory");
            h0 = mb_ld(rs, ho); h1 = mb_ld(rs, ho + 128);
            if (__all(gacc(gacc(0u, h0, T), h1, T) == 0u)) break;
            __builtin_amdgcn_s_sleep(8);
          }
          hf = v4u{h0[0], h0[2], h1[0], h1[2]};
        }
        tile_tr(bt, r1);
#pragma unroll
        for (int e = 0; e < 8; ++e) acc[e] = mfma(hf, bt[e], v4f{0.f, 0.f, 0.f, 0.f});
        wtile_out(acc, lane, o);
        const unsigned off_dp = WS_DP + (m8 * 8 + ob) * 4096 + f_t * 128 + oco * 16;
#pragma unroll
        for (int pr = 0; pr < 4; ++pr) mb_st(rs, off_dp + pr * (64 * 4096), gran(o[2 * pr], o[2 * pr + 1], T));
        if (lane < 8) cnt_add(ws, WS_C3 + ((m8 * 8 + lane) * 8 + 4) * 128);
      } else {
        T = get_tag(lds);
      }
    grp_wait<LSLEEP>(lds, G_ARRIVE, 2u);
    float gate;
    {
      unsigned c0, c1;
      cnt_wait(mksrd(ws, WS_END), WS_C3 + ((m8 * 8 + g) * 8 + 4) * 128, lane, 1, T[0] + 28u, c0, c1);
      const unsigned dof = WS_DP + w * 4096 + lane * 16;
      const bool d3live = lane < 32;
      v4u d[4];
#pragma nounroll
      for (unsigned spin = 0; spin < SPIN_CAP; ++spin) {
        asm volatile("" ::: "memory");
#pragma unroll
        for (int k = 0; k < 3; ++k) d[k] = mb_ld(rs, dof + k * 1024);
        d[3] = mb_ld(rs, d3live ? dof + 3072 : dof);
        if (gall(d, T)) break;
      }
      CNT_KEEP(c0, c1);
      const float v0 = (uf(d[0][0]) + uf(d[1][0])) + (uf(d[2][0]) + (d3live ? uf(d[3][0]) : 0.f));
      const float v1 = (uf(d[0][2]) + uf(d[1][2])) + (uf(d[2][2]) + (d3live ? uf(d[3][2]) : 0.f));
      float t = swap16_add(v0, v1);
      t = swap32_add(t, t);
      t += dpp<ROR8>(t);
      t = rbf(t);
      gate = rbf(t / (1.f + __expf(-t)));
    }
    *(float*)(lds + L_GATE + lane * 4) = gate;
    lds_set(lds, SW_GATE);
      return;
    }
  }
  {
    const int tt = is_o;
    v4u bt[8], r3[8]; v4f acc[8];
    grp_wait<LSLEEP>(lds, G_VISSUED, 4u);
    {
      const rsrc_t rs3 = mkrs(p.wo + (size_t)((4 * g + k4) * 128 + 64 * tt) * DM + 64 * m8, 63 * 1024 + 128);
#pragma unroll
      for (int t = 0; t < 8; ++t) r3[t] = __builtin_amdgcn_raw_buffer_load_b128(rs3, wt_row * 1024 + oco * 16, t * 1024, AUX_NT);
    }
    const unsigned lo = WS_OP + (g * 32 + (lane >> 3) * 4) * 2560 + k4 * 640 + 256;
    const unsigned oo = WS_OP + (g * 32 + (lane >> 3) * 4) * 2560 + k4 * 640 + tt * 384 + (lane & 7) * 16;
    const int asrc = (ab * 8 + 4 * aks + x4) * 4;
    const unsigned off_sp = WS_SP + (m8 * 8 + ob) * 4096 + (4 * g + k4) * 128 + oco * 16;
    SCHED_FENCE();
    unsigned c0, c1;
    cnt_wait(mksrd(ws, WS_END), WS_C2 + (g * 32 + (j & (C2REP - 1))) * 128, lane, 1, T[0] + 32u, c0, c1, K2);
    if (lds_peek(lds, SW_ATTDONE) == 0u) { lds_spin(lds, SW_ATTDONE, 1u); __builtin_amdgcn_s_sleep(D2); }
    v4u v[8], l[4];
#pragma unroll
    for (int k = 0; k < 4; ++k) l[k] = __builtin_amdgcn_raw_buffer_load_b128(rs, lo, k * 2560, AUX_SC1);
#pragma unroll
    for (int k = 0; k < 8; ++k) v[k] = __builtin_amdgcn_raw_buffer_load_b128(rs, oo + (k & 1) * 128, (k >> 1) * 2560, AUX_SC1);
    SCHED_FENCE();
    tile_tr(bt, r3);
#pragma unroll
    for (int e = 0; e < 8; ++e) asm volatile("" : "+v"(bt[e]));
    SCHED_FENCE();
#pragma nounroll
    for (unsigned spin = 0; spin < SPIN_CAP; ++spin) {
      if (gall(v, T, gacc(gacc(gacc(gacc(0u, l[0], T), l[1], T), l[2], T), l[3], T))) break;
      asm volatile("" ::: "memory");
#pragma unroll
      for (int k = 0; k < 4; ++k) l[k] = __builtin_amdgcn_raw_buffer_load_b128(rs, lo, k * 2560, AUX_SC1);
#pragma unroll
      for (int k = 0; k < 8; ++k) v[k] = __builtin_amdgcn_raw_buffer_load_b128(rs, oo + (k & 1) * 128, (k >> 1) * 2560, AUX_SC1);
    }
    CNT_KEEP(c0, c1);
    const float lm = fmaxf(fmaxf(uf(l[0][0]), uf(l[1][0])), fmaxf(uf(l[2][0]), uf(l[3][0])));
    float e0 = uf(l[0][2]) * __builtin_amdgcn_exp2f((uf(l[0][0]) - lm) * LOG2E), e1 = uf(l[1][2]) * __builtin_amdgcn_exp2f((uf(l[1][0]) - lm) * LOG2E);
    float e2 = uf(l[2][2]) * __builtin_amdgcn_exp2f((uf(l[2][0]) - lm) * LOG2E), e3 = uf(l[3][2]) * __builtin_amdgcn_exp2f((uf(l[3][0]) - lm) * LOG2E);
    { const float inv = __builtin_amdgcn_rcpf((e0 + e1) + (e2 + e3)); e0 *= inv; e1 *= inv; e2 *= inv; e3 *= inv; }
    v4u af;
#pragma unroll
    for (int c = 0; c < 4; ++c) {
      const int hf = c >> 1, wd_ = (c & 1) * 2;
      const unsigned a0 = v[hf][wd_], a1 = v[2 + hf][wd_], a2 = v[4 + hf][wd_], a3 = v[6 + hf][wd_];
      af[c] = __builtin_amdgcn_ds_bpermute(asrc, pk2(fmaf(e0, bflo(a0), e1 * bflo(a1)) + fmaf(e2, bflo(a2), e3 * bflo(a3)), fmaf(e0, bfhi(a0), e1 * bfhi(a1)) + fmaf(e2, bfhi(a2), e3 * bfhi(a3))));
    }
#pragma unroll
    for (int e = 0; e < 8; ++e) acc[e] = mfma(af, bt[e], v4f{0.f, 0.f, 0.f, 0.f});
    float o[8];
    wtile_out(acc, lane, o);
    *(v4f*)(lds + L_U + tt * 1024 + lane * 16) = tt ? v4f{o[0], o[1], o[2], o[3]} : v4f{o[4], o[5], o[6], o[7]};
    grp_set(lds, G_U, tt);
    grp_wait<0>(lds, G_U, 2u);
    {
      const v4f u = *(const v4f*)(lds + L_U + (1 - tt) * 1024 + lane * 16);
      const v4f m = tt ? v4f{o[4], o[5], o[6], o[7]} : v4f{o[0], o[1], o[2], o[3]};
      mb_st(rs, off_sp + (2 * tt) * (64 * 4096), gran(m[0] + u[0], m[1] + u[1], T));
      mb_st(rs, off_sp + (2 * tt + 1) * (64 * 4096), gran(m[2] + u[2], m[3] + u[3], T));
      if (tt == 0) {
        if (lane < 8) cnt_add(ws, WS_C3 + ((m8 * 8 + lane) * 8) * 128);
        lds_set(lds, SW_WODONE);
      }
    }
  }
}

extern "C" void kernel_launch(void* const* d_in, const int* in_sizes, int n_in, void* d_out, int out_size, void* d_ws, size_t ws_size, hipStream_t stream) {
  Params p;
  p.x = (const u16*)d_in[0]; p.kc = (const u16*)d_in[1]; p.vc = (const u16*)d_in[2]; p.wq = (const u16*)d_in[3]; p.wo = (const u16*)d_in[4];
  p.wg = (const u16*)d_in[6]; p.wu = (const u16*)d_in[7]; p.wd = (const u16*)d_in[8];
  p.out = (u16*)d_out;
  p.ws = (unsigned char*)d_ws + ((ws_size - (16u << 20)) & ~(size_t)0xfffff);
  (void)hipFuncSetAttribute((const void*)fused_decode, hipFuncAttributeMaxDynamicSharedMemorySize, LDS_BYTES);
  fused_decode<<<NWG, NTHR, LDS_BYTES, stream>>>(p);
}
```

```cpp
#define PROBE 3
#define PROBE_WAVE 1
#include <hip/hip_runtime.h>
#include <hip/hip_bf16.h>
#include <stdint.h>

#define STAMPS 0
#define TWIN 2
#define C2REP 8
#define Q_DELAY 16
#define G_DELAY 24
#define WO_LATE 0
#define GO_AT 0
#define F_DELAY 6
#define C3SH 1
#define H_AT G_VISSUED
#define H_CNT 4u
#define WO_TIME 750
#define KV_IL 2
#define SWZ 1
#define K2 4u
#define K3 8u
#define WO_CNT 0u
#define WO_ARR 2u
#define WD_LATE 0
#define EARLYPOLL 1
#define D2 5
#define D3 5
#define TWIN2 6
#define FASTLN 0
#define LSLEEP 1
#define F_LATE 0
#define PFN 4
#define CODE_LINES 112

typedef unsigned short u16;
typedef unsigned long long u64;
typedef unsigned v4u __attribute__((ext_vector_type(4)));
typedef unsigned v2u __attribute__((ext_vector_type(2)));
typedef float v4f __attribute__((ext_vector_type(4)));
typedef __bf16 bf16x8 __attribute__((ext_vector_type(8)));
typedef __bf16 bf16x2 __attribute__((ext_vector_type(2)));
typedef float v2f __attribute__((ext_vector_type(2)));

constexpr int DM = 512, HQ = 32, HKV = 8, SK = 4096, DH = 128, WIN = 1024, FF = 1792;
constexpr int NWG = 256, NWAVE = 12, NTHR = NWAVE * 64;
constexpr int W_C = 0, W_Q = 1, W_F = 2, W_O = 3, W_K0 = 4, W_V0 = 8;

constexpr unsigned WS_EPOCH = 0;
constexpr unsigned WS_LSE = 4096;
constexpr unsigned WS_C2 = WS_LSE + 16384;
constexpr unsigned WS_C3 = WS_C2 + 256 * 128;
constexpr unsigned WS_QP = WS_C3 + 512 * 128;
constexpr unsigned WS_GU = WS_QP + 256 * 4096;
constexpr unsigned WS_OP = WS_GU + 224 * 4096;
constexpr unsigned WS_HB = WS_OP + 256 * 2560;
constexpr unsigned WS_SP = WS_HB + 32 * 2048;
constexpr unsigned WS_DP = WS_SP + 256 * 4096;
constexpr unsigned WS_END = WS_DP + 256 * 4096;
constexpr unsigned WS_STAMP = 8u << 20;

constexpr unsigned OOB = 0x7fffff00u;
constexpr u64 EPOCH_STEP = 0x9E3779B97F4A7C15ull;
constexpr unsigned SPIN_CAP = 1u << 18;
constexpr unsigned HINT_CAP = 128;
constexpr int AUX_SC1 = 16, AUX_NT = 2;

constexpr int L_W = 0;
constexpr int L_G = 128;
constexpr int L_T = 256;
constexpr int L_MX = 272;
constexpr int L_LS = L_MX + 64;
constexpr int L_Q = L_LS + 64;
constexpr int L_U = L_Q + 1024;
constexpr int L_P = L_U + 2048;
constexpr int L_O = L_P + 8192;
constexpr int XN_STRIDE = 1040;
constexpr int L_XN = L_O + 4 * 2048;
constexpr int L_GATE = L_XN + 8 * XN_STRIDE;
constexpr int L_END = L_GATE + 256;
#define LDSKB 96
constexpr int LDS_BYTES = LDSKB * 1024;
static_assert(L_Q % 16 == 0 && L_END <= LDS_BYTES, "lds");
enum { SW_QISSUED, SW_TREADY, SW_QPUB, SW_GO, SW_FISSUED, SW_KISSUED, SW_VISSUED, SW_QREADY, SW_MAXES, SW_PREADY, SW_ARRIVE, SW_ATTDONE, SW_WODONE, SW_FPUB, SW_UREADY, SW_XN };

__device__ __forceinline__ float bflo(unsigned w) { return __uint_as_float(w << 16); }
__device__ __forceinline__ float bfhi(unsigned w) { return __uint_as_float(w & 0xffff0000u); }
__device__ __forceinline__ unsigned pk2(float lo, float hi) {
  v2f f = {lo, hi};
  bf16x2 b = __builtin_convertvector(f, bf16x2);
  return __builtin_bit_cast(unsigned, b);
}
__device__ __forceinline__ float rbf(float x) { return __uint_as_float(pk2(x, 0.f) << 16); }
__device__ __forceinline__ v4f mfma(v4u a, v4u b, v4f c) {
  return __builtin_amdgcn_mfma_f32_16x16x32_bf16(__builtin_bit_cast(bf16x8, a), __builtin_bit_cast(bf16x8, b), c, 0, 0, 0);
}
template <int CTRL> __device__ __forceinline__ float dpp(float x) {
  return __builtin_bit_cast(float, __builtin_amdgcn_mov_dpp(__builtin_bit_cast(int, x), CTRL, 0xf, 0xf, true));
}
constexpr int XOR1 = 0xB1, ROR8 = 0x128, ROR4 = 0x124;
__device__ __forceinline__ float swap16_add(float a, float b) {
  auto s = __builtin_amdgcn_permlane16_swap(__float_as_uint(a), __float_as_uint(b), false, false);
  return __uint_as_float(s[0]) + __uint_as_float(s[1]);
}
__device__ __forceinline__ float swap32_add(float a, float b) {
  auto s = __builtin_amdgcn_permlane32_swap(__float_as_uint(a), __float_as_uint(b), false, false);
  return __uint_as_float(s[0]) + __uint_as_float(s[1]);
}
__device__ __forceinline__ float xrow_sum(float x) { x = swap16_add(x, x); return swap32_add(x, x); }
__device__ __forceinline__ float xrow_max(float x) {
  auto s = __builtin_amdgcn_permlane16_swap(__float_as_uint(x), __float_as_uint(x), false, false);
  x = fmaxf(__uint_as_float(s[0]), __uint_as_float(s[1]));
  auto t = __builtin_amdgcn_permlane32_swap(__float_as_uint(x), __float_as_uint(x), false, false);
  return fmaxf(__uint_as_float(t[0]), __uint_as_float(t[1]));
}
#define BARRIER() do { asm volatile("s_waitcnt lgkmcnt(0)" ::: "memory"); __builtin_amdgcn_s_barrier(); asm volatile("" ::: "memory"); } while (0)
#define SCHED_FENCE() __builtin_amdgcn_sched_barrier(0)

typedef __amdgpu_buffer_rsrc_t rsrc_t;
__device__ __forceinline__ rsrc_t mkrs(const void* p, unsigned bytes) { return __builtin_amdgcn_make_buffer_rsrc((void*)p, 0, bytes, 0x00020000); }
__device__ __forceinline__ v4u mb_ld(rsrc_t rs, unsigned off) { return __builtin_amdgcn_raw_buffer_load_b128(rs, off, 0, AUX_SC1); }
__device__ __forceinline__ void mb_st(rsrc_t rs, unsigned off, v4u v) { __builtin_amdgcn_raw_buffer_store_b128(v, rs, off, 0, AUX_SC1); }
__device__ __forceinline__ void mb_st1(rsrc_t rs, unsigned off, unsigned v) { __builtin_amdgcn_raw_buffer_store_b32(v, rs, off, 0, AUX_SC1); }
__device__ __forceinline__ float uf(unsigned u) { return __uint_as_float(u); }

__device__ __forceinline__ void tile_tr(v4u (&bt)[8], const v4u (&w)[8]) {
#pragma unroll
  for (int u = 0; u < 4; ++u)
#pragma unroll
    for (int c = 0; c < 4; ++c) {
      bt[2 * c][u] = __builtin_amdgcn_perm(w[2 * u + 1][c], w[2 * u][c], 0x05040100u);
      bt[2 * c + 1][u] = __builtin_amdgcn_perm(w[2 * u + 1][c], w[2 * u][c], 0x07060302u);
    }
}
__device__ __forceinline__ v4u gran(float d0, float d1, v2u T) { return v4u{__float_as_uint(d0), T[0], __float_as_uint(d1), T[1]}; }
__device__ __forceinline__ unsigned gacc(unsigned acc, v4u v, v2u T) {
  acc = __builtin_amdgcn_bitop3_b32(acc, v[1], T[0], 0xF6);
  return __builtin_amdgcn_bitop3_b32(acc, v[3], T[1], 0xF6);
}
template <int N> __device__ __forceinline__ bool gall(const v4u (&v)[N], v2u T, unsigned acc = 0u) {
#pragma unroll
  for (int k = 0; k < N; ++k) acc = gacc(acc, v[k], T);
  return __all(acc == 0u);
}
__device__ __forceinline__ void wtile_out(const v4f (&acc)[8], int lane, float (&o)[8]) {
  const bool ks = (lane & 8) != 0;
#pragma unroll
  for (int e = 0; e < 8; ++e) {
    float a0 = acc[e][0], a1 = acc[e][1], a2 = acc[e][2], a3 = acc[e][3];
    asm("" : "+v"(a1), "+v"(a3));
    const float send = ks ? a1 : a2;
    const float keep = ks ? a3 : a0;
    o[e] = keep + dpp<ROR8>(send);
  }
}
__device__ __forceinline__ void sum4(const v4u (&v)[16], float (&o)[8]) {
#pragma unroll
  for (int pr = 0; pr < 4; ++pr) {
    o[2 * pr] = (uf(v[pr][0]) + uf(v[4 + pr][0])) + (uf(v[8 + pr][0]) + uf(v[12 + pr][0]));
    o[2 * pr + 1] = (uf(v[pr][2]) + uf(v[4 + pr][2])) + (uf(v[8 + pr][2]) + uf(v[12 + pr][2]));
  }
}

typedef __attribute__((address_space(3))) unsigned lds_u32;
__device__ __forceinline__ void lds_set(unsigned char* lds, int word) {
  asm volatile("s_waitcnt lgkmcnt(0)" ::: "memory");
  __hip_atomic_store((lds_u32*)(lds + L_W) + word, 1u, __ATOMIC_RELAXED, __HIP_MEMORY_SCOPE_WORKGROUP);
}
__device__ __forceinline__ void lds_wait(unsigned char* lds, int word, unsigned want) {
#pragma nounroll
  for (unsigned spin = 0; spin < (SPIN_CAP << 4); ++spin) {
    const unsigned v = __hip_atomic_load((lds_u32*)(lds + L_W) + word, __ATOMIC_RELAXED, __HIP_MEMORY_SCOPE_WORKGROUP);
    if (__builtin_amdgcn_readfirstlane(v) >= want) break;
    __builtin_amdgcn_s_sleep(LSLEEP);
  }
  asm volatile("" ::: "memory");
}
__device__ __forceinline__ unsigned lds_peek(unsigned char* lds, int word) {
  return __builtin_amdgcn_readfirstlane(__hip_atomic_load((lds_u32*)(lds + L_W) + word, __ATOMIC_RELAXED, __HIP_MEMORY_SCOPE_WORKGROUP));
}
__device__ __forceinline__ void lds_spin(unsigned char* lds, int word, unsigned want) {
#pragma nounroll
  for (unsigned spin = 0; spin < (SPIN_CAP << 5); ++spin) { if (lds_peek(lds, word) >= want) break; }
  asm volatile("" ::: "memory");
}
enum { G_KISSUED, G_VISSUED, G_MAXES, G_PREADY, G_ARRIVE, G_XN0, G_XN1, G_U };
typedef __attribute__((address_space(3))) v4u lds_v4u;
__device__ __forceinline__ void grp_set(unsigned char* lds, int grp, int i) {
  asm volatile("s_waitcnt lgkmcnt(0)" ::: "memory");
  __hip_atomic_store((lds_u32*)(lds + L_G + grp * 16) + i, 1u, __ATOMIC_RELAXED, __HIP_MEMORY_SCOPE_WORKGROUP);
}
__device__ __forceinline__ unsigned grp_count(unsigned char* lds, int grp) {
  const v4u f = *(volatile lds_v4u*)(lds + L_G + grp * 16);
  return __builtin_amdgcn_readfirstlane((f[0] + f[1]) + (f[2] + f[3]));
}
template <int SLEEP> __device__ __forceinline__ void grp_wait(unsigned char* lds, int grp, unsigned want) {
#pragma nounroll
  for (unsigned spin = 0; spin < (SPIN_CAP << 5); ++spin) { if (grp_count(lds, grp) >= want) break; if (SLEEP) __builtin_amdgcn_s_sleep(SLEEP); }
  asm volatile("" ::: "memory");
}
__device__ __forceinline__ unsigned lds_arrive(unsigned char* lds, int word, int lane) {
  asm volatile("s_waitcnt lgkmcnt(0)" ::: "memory");
  unsigned old = 0;
  if (lane == 0) old = __hip_atomic_fetch_add((lds_u32*)(lds + L_W) + word, 1u, __ATOMIC_RELAXED, __HIP_MEMORY_SCOPE_WORKGROUP);
  old = __builtin_amdgcn_readfirstlane(old);
  asm volatile("" ::: "memory");
  return old;
}
constexpr int XOR2 = 0x4E, HMIR = 0x141;
__device__ __forceinline__ float wave_sum(float x) {
  x += dpp<XOR1>(x); x += dpp<XOR2>(x); x += dpp<HMIR>(x); x += dpp<ROR8>(x);
  return xrow_sum(x);
}
__device__ __forceinline__ v4u ln_row(v4u xr) {
  float xv[8];
#pragma unroll
  for (int c = 0; c < 4; ++c) { xv[2 * c] = bflo(xr[c]); xv[2 * c + 1] = bfhi(xr[c]); }
  const float mean = wave_sum(((xv[0] + xv[1]) + (xv[2] + xv[3])) + ((xv[4] + xv[5]) + (xv[6] + xv[7]))) * (1.f / DM);
  float q = 0.f;
#pragma unroll
  for (int c = 0; c < 8; ++c) { const float d = xv[c] - mean; q = fmaf(d, d, q); }
  const float var = wave_sum(q) * (1.f / DM);
  const float mb = rbf(mean);
  const float den = rbf(sqrtf(rbf(rbf(var) + 1e-5f)));
  const float r = 1.f / den;
  v4u o;
#pragma unroll
  for (int k = 0; k < 4; ++k) {
    const unsigned n2 = pk2(xv[2 * k] - mb, xv[2 * k + 1] - mb);
    const float n0 = bflo(n2), n1 = bfhi(n2);
    const float t0 = n0 * r, t1 = n1 * r;
    o[k] = pk2(fmaf(fmaf(-t0, den, n0), r, t0), fmaf(fmaf(-t1, den, n1), r, t1));
  }
  return o;
}
__device__ __forceinline__ void cnt_add(unsigned char* ws, unsigned off) {
  __hip_atomic_fetch_add((unsigned*)(ws + off), 1u, __ATOMIC_RELAXED, __HIP_MEMORY_SCOPE_AGENT);
}
#define CNT_PEEK(r) "v_mov_b32 " r ", 0\n\tbuffer_atomic_add " r ", %2, %3, 0 offen sc0"
__device__ __forceinline__ void cnt_wait(v4u srd, unsigned off, int lane, int n, unsigned want, unsigned& r0, unsigned& r1, unsigned slack = 0u) {
  want -= slack;
  const bool idle = lane >= n;
  off = idle ? OOB : off;
  r1 = 0u;
  asm volatile(CNT_PEEK("%0") : "=&v"(r0), "+v"(r1) : "v"(off), "s"(srd) : "memory");
  __builtin_amdgcn_s_sleep(2);
#pragma nounroll
  for (unsigned spin = 0; spin < HINT_CAP; ++spin) {
    asm volatile(CNT_PEEK("%1") "\n\ts_waitcnt vmcnt(1)" : "+v"(r0), "+v"(r1) : "v"(off), "s"(srd) : "memory");
    if (__all(idle || r0 - want <= slack)) break;
    asm volatile(CNT_PEEK("%0") "\n\ts_waitcnt vmcnt(1)" : "+v"(r0), "+v"(r1) : "v"(off), "s"(srd) : "memory");
    if (__all(idle || r1 - want <= slack)) break;
  }
}
#define CNT_KEEP(r0, r1) asm volatile("" :: "v"(r0), "v"(r1))
__device__ __forceinline__ v4u mksrd(const void* p, unsigned bytes) {
  const u64 a = (u64)p;
  return v4u{(unsigned)a, (unsigned)(a >> 32) & 0xffffu, bytes, 0x00020000u};
}
__device__ __forceinline__ v2u get_tag(unsigned char* lds) {
  lds_wait(lds, SW_TREADY, 1u);
  const v2u t = *(const v2u*)(lds + L_T);
  return v2u{(unsigned)__builtin_amdgcn_readfirstlane(t[0]), (unsigned)__builtin_amdgcn_readfirstlane(t[1])};
}
struct Params {
  const u16* x; const u16* kc; const u16* vc; const u16* wq; const u16* wo; const u16* wg; const u16* wu; const u16* wd;
  u16* out; unsigned char* ws;
};

#define STAMP(i) do { st[i] = __builtin_amdgcn_s_memrealtime(); } while (0)
#define STAMP_FLUSH() do { if (wave == PROBE_WAVE) { const u64 x_ = st[PROBE] - st[0]; const u64 e_ = st[0] + 2000 + 10 * x_; while (__builtin_amdgcn_s_memrealtime() < e_) __builtin_amdgcn_s_sleep(1); } } while (0)

__global__ void __launch_bounds__(NTHR, 3) fused_decode(Params p) {
  extern __shared__ __attribute__((aligned(16))) unsigned char lds[];
  const int tid = threadIdx.x, lane = tid & 63;
  const int wave = __builtin_amdgcn_readfirstlane(tid >> 6);
  u64 st[8] = {0, 0, 0, 0, 0, 0, 0, 0};
  STAMP(0);
  u64 pc0; asm volatile("s_getpc_b64 %0" : "=s"(pc0));
  const u64 t_start = __builtin_amdgcn_s_memrealtime();
  if (wave == W_O && (blockIdx.x >> 3) < PFN) {
    unsigned pf0, pf1;
    const unsigned char* cp = (const unsigned char*)(pc0 & ~127ull) + lane * 128;
    asm volatile("global_load_dword %0, %2, off\n\tglobal_load_dword %1, %3, off\n\ts_barrier\n\ts_waitcnt vmcnt(0)" : "=&v"(pf0), "=&v"(pf1) : "v"(cp), "v"(cp + (CODE_LINES - 64) * 128) : "memory");
  } else {
    if (tid < 64) *(unsigned*)(lds + L_W + tid * 4) = 0u;
    BARRIER();
  }
  v4u xr = {0u, 0u, 0u, 0u};
  if (wave >= W_K0) xr = *(const v4u*)(p.x + (wave - W_K0) * DM + lane * 8);
  if (wave == W_C) {
    u64 E; asm volatile("s_load_dwordx2 %0, %1, 0x0\n\ts_waitcnt lgkmcnt(0)" : "=s"(E) : "s"(p.ws) : "memory");
    const u64 T64 = E + EPOCH_STEP;
    if (lane == 0) *(v2u*)(lds + L_T) = v2u{(unsigned)T64, (unsigned)(T64 >> 32)};
    lds_set(lds, SW_TREADY);
    STAMP(1);
  }
  SCHED_FENCE();
  const int w = blockIdx.x, g = w & 7, j = w >> 3;
  const int n16 = lane & 15, x4 = lane >> 4;
  const int ob = 2 * x4 + (n16 >> 3), oco = n16 & 7;
  const int ab = 2 * (n16 >> 2) + ((n16 & 3) >> 1), aks = n16 & 1;
  const int k4 = j >> 3, m8 = j & 7;
  const int f_t = k4 * 8 + g;
  const bool has_f = f_t < 28;
  const int a_b = j >> 2, a_s = j & 3;
  const bool is_q = wave == W_Q, is_f = wave == W_F, is_o = wave == W_O;
  constexpr float LOG2E = 1.4426950408889634f;
  const unsigned wt_row = 32 * (n16 >> 3) + 8 * x4;

  if (wave >= W_K0) {
    {
      const int row = wave - W_K0;
      *(v4u*)(lds + L_XN + row * XN_STRIDE + lane * 16) = ln_row(xr);
      grp_set(lds, G_XN0 + (row >> 2), row & 3);
    }
    unsigned char* ws = p.ws; asm volatile("; role A" : "+s"(ws));
    const rsrc_t rs = mkrs(ws, WS_END);
    const bool is_k = wave < W_V0;
    const int a = (wave - W_K0) & 3;
    const size_t kvb = ((size_t)(a_b * HKV + g) * SK + (SK - WIN) + (8 * a + ((a_s + 2 * a) & 3)) * 32) * (DH * 2);
    const unsigned T2 = (4 + ((a_s + 2 * a + 1) & 3) - ((a_s + 2 * a) & 3)) * 8192;
    const int blk = 2 * a + (is_k ? 0 : 1);
    const unsigned off_op = lane < (is_k ? 17 : 16) ? WS_OP + (g * 32 + j) * 2560 + a * 640 + (is_k ? 0 : 384) + lane * 16 : OOB;
    float lse = 0.f;
    const unsigned char* osrc = lds + L_O + ((lane >> 3) & 1) * 1024 + (blk * 8 + (lane & 7)) * 16;
    float inv;
    v2u T;
    if (is_k) {
      v4u r[16];
      {
        const rsrc_t rs1 = mkrs((const unsigned char*)p.kc + kvb, T2 + 8192);
        const unsigned vo = (n16 >> 2) * 256 + x4 * 64 + (n16 & 3) * 16;
        SCHED_FENCE(); lds_wait(lds, SW_FISSUED, 1u); SCHED_FENCE();
#pragma unroll
        for (int t = 0; t < 16; ++t) {
          if (t == TWIN) { lds_spin(lds, TWIN2 > TWIN ? SW_QPUB : SW_GO, 1u); SCHED_FENCE(); }
          if (TWIN2 > TWIN && t == TWIN2) { lds_spin(lds, SW_GO, 1u); SCHED_FENCE(); }
          r[t] = __builtin_amdgcn_raw_buffer_load_b128(rs1, vo + (t & 3) * 1024, ((t >> 2) & 1) * 4096 + (t >> 3) * T2, AUX_NT);
          SCHED_FENCE();
        }
        grp_set(lds, G_KISSUED, a);
      }
      STAMP(1);
      T = get_tag(lds);
      lds_wait(lds, SW_QREADY, 1u);
      STAMP(2);
      const v4u qf = *(const v4u*)(lds + L_Q + lane * 16);
      float sc[16];
#pragma unroll
      for (int t = 0; t < 16; t += 2) {
        const v4f d = mfma(r[t], qf, v4f{0.f, 0.f, 0.f, 0.f});
        const v4f e = mfma(r[t + 1], qf, v4f{0.f, 0.f, 0.f, 0.f});
        float v0 = dpp<0x00>(d[0]); v0 += dpp<0x55>(d[1]); v0 += dpp<0xAA>(d[2]); v0 += dpp<0xFF>(d[3]);
        float v1 = dpp<0x00>(e[0]); v1 += dpp<0x55>(e[1]); v1 += dpp<0xAA>(e[2]); v1 += dpp<0xFF>(e[3]);
        const unsigned u0 = pk2(v0, v1);
        const unsigned u1 = pk2(bflo(u0) * 0.08838834764831845f, bfhi(u0) * 0.08838834764831845f);
        sc[t] = bflo(u1); sc[t + 1] = bfhi(u1);
      }
      float mx = sc[0];
#pragma unroll
      for (int t = 1; t < 16; ++t) mx = fmaxf(mx, sc[t]);
      mx = xrow_max(mx);
      const int hs = n16 >> 2;
      if (lane < 16 && (lane & 3) == 0) *(float*)(lds + L_MX + (hs * 4 + a) * 4) = mx;
      grp_set(lds, G_MAXES, a);
      grp_wait<0>(lds, G_MAXES, 4u);
      { const v4f m0 = *(const v4f*)(lds + L_MX + hs * 16); mx = fmaxf(fmaxf(m0[0], m0[1]), fmaxf(m0[2], m0[3])); }
      const float mxl = mx * LOG2E;
      float pr[16], ls = 0.f;
#pragma unroll
      for (int t = 0; t < 16; ++t) { pr[t] = __builtin_amdgcn_exp2f(fmaf(sc[t], LOG2E, -mxl)); ls += pr[t]; }
      ls = xrow_sum(ls);
      if (lane < 16 && (lane & 3) == 0) *(float*)(lds + L_LS + (hs * 4 + a) * 4) = ls;
      *(v4u*)(lds + L_P + (2 * a) * 1024 + lane * 16) = v4u{pk2(pr[0], pr[1]), pk2(pr[2], pr[3]), pk2(pr[4], pr[5]), pk2(pr[6], pr[7])};
      *(v4u*)(lds + L_P + (2 * a + 1) * 1024 + lane * 16) = v4u{pk2(pr[8], pr[9]), pk2(pr[10], pr[11]), pk2(pr[12], pr[13]), pk2(pr[14], pr[15])};
      grp_set(lds, G_PREADY, a);
      STAMP(3);
      grp_wait<LSLEEP>(lds, G_PREADY, 4u);
      const v4f l0 = *(const v4f*)(lds + L_LS + a * 16), m0 = *(const v4f*)(lds + L_MX + a * 16);
      const float L = (l0[0] + l0[1]) + (l0[2] + l0[3]);
      inv = __builtin_amdgcn_rcpf(L);
      lse = fmaf(fmaxf(fmaxf(m0[0], m0[1]), fmaxf(m0[2], m0[3])), LOG2E, __builtin_amdgcn_logf(L));
      grp_wait<LSLEEP>(lds, G_ARRIVE, 3u);
    } else {
      v4u r[16];
      {
        const rsrc_t rs2 = mkrs((const unsigned char*)p.vc + kvb, T2 + 8192);
        SCHED_FENCE(); grp_wait<LSLEEP>(lds, G_KISSUED, 4u); SCHED_FENCE();
#pragma unroll
        for (int t = 0; t < 16; ++t) r[t] = __builtin_amdgcn_raw_buffer_load_b128(rs2, lane * 16 + (t & 3) * 1024, ((t >> 2) & 1) * 4096 + (t >> 3) * T2, AUX_NT);
        SCHED_FENCE();
        grp_set(lds, G_VISSUED, a);
      }
      STAMP(1);
      T = get_tag(lds);
      grp_wait<LSLEEP>(lds, G_PREADY, 4u);
      STAMP(2);
      const v4u p0 = *(const v4u*)(lds + L_P + (2 * a) * 1024 + lane * 16), p1 = *(const v4u*)(lds + L_P + (2 * a + 1) * 1024 + lane * 16);
      { const v4f l0 = *(const v4f*)(lds + L_LS + a * 16); inv = __builtin_amdgcn_rcpf((l0[0] + l0[1]) + (l0[2] + l0[3])); }
      asm volatile("" : "+v"(inv));
      v4f acc[8];
#pragma unroll
      for (int h = 0; h < 2; ++h) {
        v4u bt[8];
#pragma unroll
        for (int u = 0; u < 4; ++u) {
          SCHED_FENCE();
#pragma unroll
          for (int c = 0; c < 4; ++c) {
            bt[2 * c][u] = __builtin_amdgcn_perm(r[8 * h + 2 * u + 1][c], r[8 * h + 2 * u][c], 0x05040100u);
            bt[2 * c + 1][u] = __builtin_amdgcn_perm(r[8 * h + 2 * u + 1][c], r[8 * h + 2 * u][c], 0x07060302u);
          }
        }
#pragma unroll
        for (int e = 0; e < 8; ++e) acc[e] = mfma(h ? p1 : p0, bt[e], h ? acc[e] : v4f{0.f, 0.f, 0.f, 0.f});
      }
      *(v4f*)(lds + L_O + a * 2048 + lane * 16) = v4f{acc[0][0], acc[1][0], acc[2][0], acc[3][0]};
      *(v4f*)(lds + L_O + a * 2048 + 1024 + lane * 16) = v4f{acc[4][0], acc[5][0], acc[6][0], acc[7][0]};
      grp_set(lds, G_ARRIVE, a);
      STAMP(3);
    }
    grp_wait<0>(lds, G_ARRIVE, 4u);
    STAMP(4);
    {
      const v4f o0 = *(const v4f*)(osrc), o1 = *(const v4f*)(osrc + 2048), o2 = *(const v4f*)(osrc + 4096), o3 = *(const v4f*)(osrc + 6144);
      const v4f o = ((o0 + o1) + (o2 + o3)) * inv;
      mb_st(rs, off_op, v4u{lane == 16 ? __float_as_uint(lse) : pk2(o[0], o[1]), T[0], pk2(o[2], o[3]), T[1]});
      if (wave == W_K0) {
        if (lane < C2REP) cnt_add(ws, WS_C2 + (g * 32 + lane) * 128);
        lds_set(lds, SW_ATTDONE);
      }
    }
    STAMP(5);
    STAMP_FLUSH();
    return;
  }

  if (wave == W_C) {
    unsigned char* ws = p.ws; asm volatile("; role C" : "+s"(ws));
    const rsrc_t rs = mkrs(ws, WS_END);
    const v4u srd = mksrd(ws, WS_END);
    const v2u T = get_tag(lds);
    const unsigned co = WS_C3 + ((m8 * 8 + g) * 8) * 128;
    {
      const unsigned so = lane == 0 ? (j < C2REP ? WS_C2 + (g * 32 + j) * 128 : OOB) : (lane < 6 && k4 == 0) ? co + (lane - 1) * 128 : OOB;
      mb_st1(rs, so, T[0]);
    }
    const int fidx = g * DM + 64 * m8 + 8 * (lane & 7) + 2 * k4 + (x4 & 1);
    lds_wait(lds, SW_QPUB, 1u);
    __builtin_amdgcn_s_sleep(Q_DELAY);
    STAMP(3);
    {
      const int qcb = 2 * (n16 >> 2) + (x4 >> 1);
      const unsigned qo = WS_QP + (g * 32 + qcb) * 4096 + ((a_b + SWZ * qcb) & 7) * 128 + (4 * (x4 & 1) + (n16 & 3)) * 16;
      v4u v[16];
#pragma nounroll
      for (unsigned spin = 0; spin < SPIN_CAP; ++spin) {
        asm volatile("" ::: "memory");
#pragma unroll
        for (int k = 0; k < 16; ++k) v[k] = __builtin_amdgcn_raw_buffer_load_b128(rs, qo + (k & 3) * 1024, (k >> 2) * 32768, AUX_SC1);
        SCHED_FENCE(); lds_set(lds, SW_GO); SCHED_FENCE();
        if (gall(v, T)) break;
      }
      float q[8];
      sum4(v, q);
      *(v4u*)(lds + L_Q + lane * 16) = v4u{pk2(q[0], q[1]), pk2(q[2], q[3]), pk2(q[4], q[5]), pk2(q[6], q[7])};
    }
    lds_set(lds, SW_QREADY);
    STAMP(4);
    const float xres = bflo(p.x[fidx]);
    {
      unsigned c0, c1;
      lds_wait(lds, SW_ATTDONE, 1u);
      __builtin_amdgcn_s_sleep(24);
      cnt_wait(srd, co + lane * 128, lane, C3SH, T[0] + 32u / C3SH, c0, c1, K3);
      if (lds_peek(lds, SW_WODONE) == 0u) { lds_spin(lds, SW_WODONE, 1u); __builtin_amdgcn_s_sleep(D3); }
      STAMP(6);
      const unsigned so = WS_SP + w * 4096 + lane * 16;
      v4u s[4];
#pragma nounroll
      for (unsigned spin = 0; spin < SPIN_CAP; ++spin) {
        asm volatile("" ::: "memory");
#pragma unroll
        for (int k = 0; k < 4; ++k) s[k] = mb_ld(rs, so + k * 1024);
        if (gall(s, T)) break;
      }
      CNT_KEEP(c0, c1);
      const float v0 = (uf(s[0][0]) + uf(s[1][0])) + (uf(s[2][0]) + uf(s[3][0]));
      const float v1 = (uf(s[0][2]) + uf(s[1][2])) + (uf(s[2][2]) + uf(s[3][2]));
      float t = swap16_add(v0, v1);
      t = swap32_add(t, t);
      t += dpp<ROR8>(t);
      lds_spin(lds, SW_XN, 1u);
      const float gate = *(const float*)(lds + L_GATE + lane * 4);
      const float r = rbf(rbf(t) * gate);
      if (lane < 32 && n16 < 8) p.out[fidx] = (u16)pk2(xres + r, 0.f);
      if (w == 0 && lane == 0) __builtin_amdgcn_raw_buffer_store_b64(T, rs, WS_EPOCH, 0, AUX_SC1);
    }
    STAMP(7);
    STAMP_FLUSH();
    return;
  }

  unsigned char* ws = p.ws; asm volatile("; role QFO" : "+s"(ws));
  const rsrc_t rs = mkrs(ws, WS_END);
  v2u T;
  if (is_o) {
    T = get_tag(lds);
    STAMP(2);
  } else {
    v4u r1[8], r2[8];
    const int kq = is_q ? k4 : (m8 & 3);
    {
      const unsigned stride = is_q ? HQ * DH * 2 : FF * 2;
      const unsigned char* b1 = (is_q ? (const unsigned char*)(p.wq + 512 * g + 64 * m8) : (const unsigned char*)((m8 >> 2 ? p.wu : p.wg) + 64 * f_t)) + (size_t)(128 * kq) * stride;
      const rsrc_t rs1 = mkrs(b1, (is_q || has_f) ? 127 * stride + 128 : 0u);
      const unsigned vo1 = wt_row * stride + oco * 16;
      SCHED_FENCE();
      if (is_f) { lds_wait(lds, SW_QISSUED, 1u); __builtin_amdgcn_s_sleep(F_DELAY); }
      SCHED_FENCE();
#pragma unroll
      for (int t = 0; t < 8; ++t) r1[t] = __builtin_amdgcn_raw_buffer_load_b128(rs1, vo1, t * stride, AUX_NT);
      SCHED_FENCE();
#pragma unroll
      for (int t = 0; t < 8; ++t) r2[t] = __builtin_amdgcn_raw_buffer_load_b128(rs1, vo1, (64 + t) * stride, AUX_NT);
      SCHED_FENCE();
      lds_set(lds, is_q ? SW_QISSUED : SW_FISSUED);
      SCHED_FENCE();
    }
    STAMP(1);
    v4f acc[8];
    float o[8];
    v4u bt[8];
    if (is_q || has_f) {
      T = get_tag(lds);
      grp_wait<0>(lds, G_XN0, 4u); grp_wait<0>(lds, G_XN1, 4u);
      const unsigned char* xa = lds + L_XN + ab * XN_STRIDE + (128 * kq + 32 * aks + 8 * x4) * 2;
      const v4u a0 = *(const v4u*)xa, a1 = *(const v4u*)(xa + 128);
      STAMP(2);
      tile_tr(bt, r1);
#pragma unroll
      for (int e = 0; e < 8; ++e) acc[e] = mfma(a0, bt[e], v4f{0.f, 0.f, 0.f, 0.f});
      tile_tr(bt, r2);
#pragma unroll
      for (int e = 0; e < 8; ++e) acc[e] = mfma(a1, bt[e], acc[e]);
      wtile_out(acc, lane, o);
      const unsigned off_a = (is_q ? WS_QP + (g * 32 + j) * 4096 : WS_GU + (f_t * 8 + m8) * 4096) + ((ob + SWZ * m8) & 7) * 128 + oco * 16;
#pragma unroll
      for (int pr = 0; pr < 4; ++pr) mb_st(rs, off_a + pr * 1024, gran(o[2 * pr], o[2 * pr + 1], T));
      lds_set(lds, is_q ? SW_QPUB : SW_FPUB);
    }
    STAMP(3);
    if (is_f) {
      if (has_f) {
        __builtin_amdgcn_s_sleep(G_DELAY);
        {
          const unsigned go = WS_GU + (f_t * 8 + (lane >> 3)) * 4096 + ((m8 + SWZ * (lane >> 3)) & 7) * 128 + (lane & 7) * 16;
          v4u v[4];
#pragma nounroll
          for (unsigned spin = 0; spin < SPIN_CAP; ++spin) {
            asm volatile("" ::: "memory");
#pragma unroll
            for (int k = 0; k < 4; ++k) v[k] = mb_ld(rs, go + k * 1024);
            if (gall(v, T)) break;
            __builtin_amdgcn_s_sleep(8);
          }
          unsigned hp[4];
#pragma unroll
          for (int k = 0; k < 4; ++k) {
            float a0 = uf(v[k][0]), a1 = uf(v[k][2]);
            a0 += dpp<ROR8>(a0); a1 += dpp<ROR8>(a1);
            a0 = swap16_add(a0, a0); a1 = swap16_add(a1, a1);
            const unsigned r2_ = pk2(a0, a1);
            auto sw = __builtin_amdgcn_permlane32_swap(r2_, r2_, false, false);
            hp[k] = pk2(bflo(sw[0]) * bflo(sw[1]), bfhi(sw[0]) * bfhi(sw[1]));
          }
          const bool hi = (lane & 8) != 0;
          mb_st(rs, lane < 16 ? WS_HB + (f_t * 8 + m8) * 256 + lane * 16 : OOB, v4u{hi ? hp[2] : hp[0], T[0], hi ? hp[3] : hp[1], T[1]});
        }
        STAMP(4);
        grp_wait<LSLEEP>(lds, H_AT, H_CNT);
        {
          const rsrc_t rs3 = mkrs(p.wd + (size_t)(64 * f_t) * DM + 64 * m8, 63 * 1024 + 128);
#pragma unroll
          for (int t = 0; t < 8; ++t) r1[t] = __builtin_amdgcn_raw_buffer_load_b128(rs3, wt_row * 1024 + oco * 16, t * 1024, AUX_NT);
        }
        SCHED_FENCE();
        v4u hf;
        {
          const unsigned ho = WS_HB + (f_t * 8 + ab) * 256 + (4 * aks + x4) * 16;
          v4u h0, h1;
#pragma nounroll
          for (unsigned spin = 0; spin < SPIN_CAP; ++spin) {
            asm volatile("" ::: "memory");
            h0 = mb_ld(rs, ho); h1 = mb_ld(rs, ho + 128);
            if (__all(gacc(gacc(0u, h0, T), h1, T) == 0u)) break;
            __builtin_amdgcn_s_sleep(8);
          }
          hf = v4u{h0[0], h0[2], h1[0], h1[2]};
        }
        tile_tr(bt, r1);
#pragma unroll
        for (int e = 0; e < 8; ++e) acc[e] = mfma(hf, bt[e], v4f{0.f, 0.f, 0.f, 0.f});
        wtile_out(acc, lane, o);
        const unsigned off_dp = WS_DP + (m8 * 8 + ob) * 4096 + f_t * 128 + oco * 16;
#pragma unroll
        for (int pr = 0; pr < 4; ++pr) mb_st(rs, off_dp + pr * (64 * 4096), gran(o[2 * pr], o[2 * pr + 1], T));
        if (lane < 8) cnt_add(ws, WS_C3 + ((m8 * 8 + lane) * 8 + 4) * 128);
        STAMP(5);
      } else {
        T = get_tag(lds);
      }
    grp_wait<LSLEEP>(lds, G_ARRIVE, 2u);
    float gate;
    {
      unsigned c0, c1;
      cnt_wait(mksrd(ws, WS_END), WS_C3 + ((m8 * 8 + g) * 8 + 4) * 128, lane, 1, T[0] + 28u, c0, c1);
      const unsigned dof = WS_DP + w * 4096 + lane * 16;
      const bool d3live = lane < 32;
      v4u d[4];
#pragma nounroll
      for (unsigned spin = 0; spin < SPIN_CAP; ++spin) {
        asm volatile("" ::: "memory");
#pragma unroll
        for (int k = 0; k < 3; ++k) d[k] = mb_ld(rs, dof + k * 1024);
        d[3] = mb_ld(rs, d3live ? dof + 3072 : dof);
        if (gall(d, T)) break;
      }
      CNT_KEEP(c0, c1);
      const float v0 = (uf(d[0][0]) + uf(d[1][0])) + (uf(d[2][0]) + (d3live ? uf(d[3][0]) : 0.f));
      const float v1 = (uf(d[0][2]) + uf(d[1][2])) + (uf(d[2][2]) + (d3live ? uf(d[3][2]) : 0.f));
      float t = swap16_add(v0, v1);
      t = swap32_add(t, t);
      t += dpp<ROR8>(t);
      t = rbf(t);
      gate = rbf(t / (1.f + __expf(-t)));
    }
    *(float*)(lds + L_GATE + lane * 4) = gate;
    lds_set(lds, SW_XN);
    STAMP(6);
      STAMP_FLUSH();
      return;
    }
  }
  {
    const int tt = is_o;
    v4u bt[8], r3[8]; v4f acc[8];
    grp_wait<LSLEEP>(lds, G_VISSUED, 4u);
    {
      const rsrc_t rs3 = mkrs(p.wo + (size_t)((4 * g + k4) * 128 + 64 * tt) * DM + 64 * m8, 63 * 1024 + 128);
#pragma unroll
      for (int t = 0; t < 8; ++t) r3[t] = __builtin_amdgcn_raw_buffer_load_b128(rs3, wt_row * 1024 + oco * 16, t * 1024, AUX_NT);
    }
    const unsigned lo = WS_OP + (g * 32 + (lane >> 3) * 4) * 2560 + k4 * 640 + 256;
    const unsigned oo = WS_OP + (g * 32 + (lane >> 3) * 4) * 2560 + k4 * 640 + tt * 384 + (lane & 7) * 16;
    const int asrc = (ab * 8 + 4 * aks + x4) * 4;
    const unsigned off_sp = WS_SP + (m8 * 8 + ob) * 4096 + (4 * g + k4) * 128 + oco * 16;
    SCHED_FENCE();
    STAMP(5);
    unsigned c0, c1;
    cnt_wait(mksrd(ws, WS_END), WS_C2 + (g * 32 + (j & (C2REP - 1))) * 128, lane, 1, T[0] + 32u, c0, c1, K2);
    if (lds_peek(lds, SW_ATTDONE) == 0u) { lds_spin(lds, SW_ATTDONE, 1u); __builtin_amdgcn_s_sleep(D2); }
    STAMP(4);
    v4u v[8], l[4];
#pragma unroll
    for (int k = 0; k < 4; ++k) l[k] = __builtin_amdgcn_raw_buffer_load_b128(rs, lo, k * 2560, AUX_SC1);
#pragma unroll
    for (int k = 0; k < 8; ++k) v[k] = __builtin_amdgcn_raw_buffer_load_b128(rs, oo + (k & 1) * 128, (k >> 1) * 2560, AUX_SC1);
    SCHED_FENCE();
    tile_tr(bt, r3);
#pragma unroll
    for (int e = 0; e < 8; ++e) asm volatile("" : "+v"(bt[e]));
    SCHED_FENCE();
#pragma nounroll
    for (unsigned spin = 0; spin < SPIN_CAP; ++spin) {
      if (gall(v, T, gacc(gacc(gacc(gacc(0u, l[0], T), l[1], T), l[2], T), l[3], T))) break;
      asm volatile("" ::: "memory");
#pragma unroll
      for (int k = 0; k < 4; ++k) l[k] = __builtin_amdgcn_raw_buffer_load_b128(rs, lo, k * 2560, AUX_SC1);
#pragma unroll
      for (int k = 0; k < 8; ++k) v[k] = __builtin_amdgcn_raw_buffer_load_b128(rs, oo + (k & 1) * 128, (k >> 1) * 2560, AUX_SC1);
    }
    CNT_KEEP(c0, c1);
    STAMP(6);
    const float lm = fmaxf(fmaxf(uf(l[0][0]), uf(l[1][0])), fmaxf(uf(l[2][0]), uf(l[3][0])));
    float e0 = __builtin_amdgcn_exp2f(uf(l[0][0]) - lm), e1 = __builtin_amdgcn_exp2f(uf(l[1][0]) - lm), e2 = __builtin_amdgcn_exp2f(uf(l[2][0]) - lm), e3 = __builtin_amdgcn_exp2f(uf(l[3][0]) - lm);
    { const float inv = __builtin_amdgcn_rcpf((e0 + e1) + (e2 + e3)); e0 *= inv; e1 *= inv; e2 *= inv; e3 *= inv; }
    v4u af;
#pragma unroll
    for (int c = 0; c < 4; ++c) {
      const int hf = c >> 1, wd_ = (c & 1) * 2;
      const unsigned a0 = v[hf][wd_], a1 = v[2 + hf][wd_], a2 = v[4 + hf][wd_], a3 = v[6 + hf][wd_];
      af[c] = __builtin_amdgcn_ds_bpermute(asrc, pk2(fmaf(e0, bflo(a0), e1 * bflo(a1)) + fmaf(e2, bflo(a2), e3 * bflo(a3)), fmaf(e0, bfhi(a0), e1 * bfhi(a1)) + fmaf(e2, bfhi(a2), e3 * bfhi(a3))));
    }
#pragma unroll
    for (int e = 0; e < 8; ++e) acc[e] = mfma(af, bt[e], v4f{0.f, 0.f, 0.f, 0.f});
    float o[8];
    wtile_out(acc, lane, o);
    *(v4f*)(lds + L_U + tt * 1024 + lane * 16) = tt ? v4f{o[0], o[1], o[2], o[3]} : v4f{o[4], o[5], o[6], o[7]};
    grp_set(lds, G_U, tt);
    grp_wait<0>(lds, G_U, 2u);
    {
      const v4f u = *(const v4f*)(lds + L_U + (1 - tt) * 1024 + lane * 16);
      const v4f m = tt ? v4f{o[4], o[5], o[6], o[7]} : v4f{o[0], o[1], o[2], o[3]};
      mb_st(rs, off_sp + (2 * tt) * (64 * 4096), gran(m[0] + u[0], m[1] + u[1], T));
      mb_st(rs, off_sp + (2 * tt + 1) * (64 * 4096), gran(m[2] + u[2], m[3] + u[3], T));
      if (tt == 0) {
        if (lane < 8) cnt_add(ws, WS_C3 + ((m8 * 8 + lane) * 8 + (g & (C3SH - 1))) * 128);
        lds_set(lds, SW_WODONE);
      }
    }
    STAMP(7);
  }
  STAMP_FLUSH();
}

extern "C" void kernel_launch(void* const* d_in, const int* in_sizes, int n_in, void* d_out, int out_size, void* d_ws, size_t ws_size, hipStream_t stream) {
  Params p;
  p.x = (const u16*)d_in[0]; p.kc = (const u16*)d_in[1]; p.vc = (const u16*)d_in[2]; p.wq = (const u16*)d_in[3]; p.wo = (const u16*)d_in[4];
  p.wg = (const u16*)d_in[6]; p.wu = (const u16*)d_in[7]; p.wd = (const u16*)d_in[8];
  p.out = (u16*)d_out;
  p.ws = (unsigned char*)d_ws + ((ws_size - (16u << 20)) & ~(size_t)0xfffff);
  (void)hipFuncSetAttribute((const void*)fused_decode, hipFuncAttributeMaxDynamicSharedMemorySize, LDS_BYTES);
  fused_decode<<<NWG, NTHR, LDS_BYTES, stream>>>(p);
}
```

```cpp
#include <hip/hip_runtime.h>
#include <hip/hip_bf16.h>
#include <stdint.h>

typedef unsigned short u16;
typedef unsigned long long u64;
typedef unsigned v4u __attribute__((ext_vector_type(4)));
typedef unsigned v2u __attribute__((ext_vector_type(2)));
typedef float v4f __attribute__((ext_vector_type(4)));
typedef __bf16 bf16x8 __attribute__((ext_vector_type(8)));
typedef __bf16 bf16x2 __attribute__((ext_vector_type(2)));
typedef float v2f __attribute__((ext_vector_type(2)));

constexpr int DM = 512, HQ = 32, HKV = 8, SK = 4096, DH = 128, WIN = 1024, FF = 1792;
constexpr int NWG = 256, NWAVE = 12, NTHR = NWAVE * 64;
constexpr int W_C = 0, W_Q = 1, W_F = 2, W_O = 3, W_K0 = 4, W_V0 = 8;

constexpr unsigned WS_EPOCH = 0;
constexpr unsigned WS_C2 = 20480;
constexpr unsigned WS_C3 = WS_C2 + 256 * 128;
constexpr unsigned WS_QP = WS_C3 + 512 * 128;
constexpr unsigned WS_GU = WS_QP + 256 * 4096;
constexpr unsigned WS_OP = WS_GU + 224 * 4096;
constexpr unsigned WS_HB = WS_OP + 256 * 2560;
constexpr unsigned WS_SP = WS_HB + 32 * 2048;
constexpr unsigned WS_DP = WS_SP + 256 * 4096;
constexpr unsigned WS_END = WS_DP + 256 * 4096;

constexpr unsigned OOB = 0x7fffff00u;
constexpr u64 EPOCH_STEP = 0x9E3779B97F4A7C15ull;
constexpr unsigned SPIN_CAP = 1u << 18;
constexpr unsigned HINT_CAP = 128;
constexpr int AUX_SC1 = 16, AUX_NT = 2;
constexpr int TWIN = 2, TWIN2 = 6;
constexpr int F_DELAY = 10, Q_DELAY = 22, G_DELAY = 24, D2 = 5, D3 = 5, LSLEEP = 1;
constexpr int C2REP = 8;
constexpr unsigned K2 = 4u, K3 = 8u;
constexpr int PFN = 4, CODE_LINES = 112;

constexpr int L_W = 0;
constexpr int L_G = 128;
constexpr int L_T = 256;
constexpr int L_MX = 272;
constexpr int L_LS = L_MX + 64;
constexpr int L_Q = L_LS + 64;
constexpr int L_U = L_Q + 1024;
constexpr int L_P = L_U + 2048;
constexpr int L_O = L_P + 8192;
constexpr int XN_STRIDE = 1040;
constexpr int L_XN = L_O + 4 * 2048;
constexpr int L_GATE = L_XN + 8 * XN_STRIDE;
constexpr int L_END = L_GATE + 256;
constexpr int LDS_BYTES = 96 * 1024;
static_assert(L_Q % 16 == 0 && L_END <= LDS_BYTES, "lds");
enum { SW_QISSUED, SW_TREADY, SW_QPUB, SW_GO, SW_FISSUED, SW_QREADY, SW_ATTDONE, SW_WODONE, SW_FPUB, SW_GATE };

__device__ __forceinline__ float bflo(unsigned w) { return __uint_as_float(w << 16); }
__device__ __forceinline__ float bfhi(unsigned w) { return __uint_as_float(w & 0xffff0000u); }
__device__ __forceinline__ unsigned pk2(float lo, float hi) {
  v2f f = {lo, hi};
  bf16x2 b = __builtin_convertvector(f, bf16x2);
  return __builtin_bit_cast(unsigned, b);
}
__device__ __forceinline__ float rbf(float x) { return __uint_as_float(pk2(x, 0.f) << 16); }
__device__ __forceinline__ v4f mfma(v4u a, v4u b, v4f c) {
  return __builtin_amdgcn_mfma_f32_16x16x32_bf16(__builtin_bit_cast(bf16x8, a), __builtin_bit_cast(bf16x8, b), c, 0, 0, 0);
}
template <int CTRL> __device__ __forceinline__ float dpp(float x) {
  return __builtin_bit_cast(float, __builtin_amdgcn_mov_dpp(__builtin_bit_cast(int, x), CTRL, 0xf, 0xf, true));
}
constexpr int XOR1 = 0xB1, ROR8 = 0x128;
__device__ __forceinline__ float swap16_add(float a, float b) {
  auto s = __builtin_amdgcn_permlane16_swap(__float_as_uint(a), __float_as_uint(b), false, false);
  return __uint_as_float(s[0]) + __uint_as_float(s[1]);
}
__device__ __forceinline__ float swap32_add(float a, float b) {
  auto s = __builtin_amdgcn_permlane32_swap(__float_as_uint(a), __float_as_uint(b), false, false);
  return __uint_as_float(s[0]) + __uint_as_float(s[1]);
}
__device__ __forceinline__ float xrow_sum(float x) { x = swap16_add(x, x); return swap32_add(x, x); }
__device__ __forceinline__ float xrow_max(float x) {
  auto s = __builtin_amdgcn_permlane16_swap(__float_as_uint(x), __float_as_uint(x), false, false);
  x = fmaxf(__uint_as_float(s[0]), __uint_as_float(s[1]));
  auto t = __builtin_amdgcn_permlane32_swap(__float_as_uint(x), __float_as_uint(x), false, false);
  return fmaxf(__uint_as_float(t[0]), __uint_as_float(t[1]));
}
#define BARRIER() do { asm volatile("s_waitcnt lgkmcnt(0)" ::: "memory"); __builtin_amdgcn_s_barrier(); asm volatile("" ::: "memory"); } while (0)
#define SCHED_FENCE() __builtin_amdgcn_sched_barrier(0)

typedef __amdgpu_buffer_rsrc_t rsrc_t;
__device__ __forceinline__ rsrc_t mkrs(const void* p, unsigned bytes) { return __builtin_amdgcn_make_buffer_rsrc((void*)p, 0, bytes, 0x00020000); }
__device__ __forceinline__ v4u mb_ld(rsrc_t rs, unsigned off) { return __builtin_amdgcn_raw_buffer_load_b128(rs, off, 0, AUX_SC1); }
__device__ __forceinline__ void mb_st(rsrc_t rs, unsigned off, v4u v) { __builtin_amdgcn_raw_buffer_store_b128(v, rs, off, 0, AUX_SC1); }
__device__ __forceinline__ void mb_st1(rsrc_t rs, unsigned off, unsigned v) { __builtin_amdgcn_raw_buffer_store_b32(v, rs, off, 0, AUX_SC1); }
__device__ __forceinline__ float uf(unsigned u) { return __uint_as_float(u); }

__device__ __forceinline__ void tile_tr(v4u (&bt)[8], const v4u (&w)[8]) {
#pragma unroll
  for (int u = 0; u < 4; ++u)
#pragma unroll
    for (int c = 0; c < 4; ++c) {
      bt[2 * c][u] = __builtin_amdgcn_perm(w[2 * u + 1][c], w[2 * u][c], 0x05040100u);
      bt[2 * c + 1][u] = __builtin_amdgcn_perm(w[2 * u + 1][c], w[2 * u][c], 0x07060302u);
    }
}
__device__ __forceinline__ v4u gran(float d0, float d1, v2u T) { return v4u{__float_as_uint(d0), T[0], __float_as_uint(d1), T[1]}; }
__device__ __forceinline__ unsigned gacc(unsigned acc, v4u v, v2u T) {
  acc = __builtin_amdgcn_bitop3_b32(acc, v[1], T[0], 0xF6);
  return __builtin_amdgcn_bitop3_b32(acc, v[3], T[1], 0xF6);
}
template <int N> __device__ __forceinline__ bool gall(const v4u (&v)[N], v2u T, unsigned acc = 0u) {
#pragma unroll
  for (int k = 0; k < N; ++k) acc = gacc(acc, v[k], T);
  return __all(acc == 0u);
}
__device__ __forceinline__ void wtile_out(const v4f (&acc)[8], int lane, float (&o)[8]) {
  const bool ks = (lane & 8) != 0;
#pragma unroll
  for (int e = 0; e < 8; ++e) {
    float a0 = acc[e][0], a1 = acc[e][1], a2 = acc[e][2], a3 = acc[e][3];
    asm("" : "+v"(a1), "+v"(a3));
    const float send = ks ? a1 : a2;
    const float keep = ks ? a3 : a0;
    o[e] = keep + dpp<ROR8>(send);
  }
}
__device__ __forceinline__ void sum4(const v4u (&v)[16], float (&o)[8]) {
#pragma unroll
  for (int pr = 0; pr < 4; ++pr) {
    o[2 * pr] = (uf(v[pr][0]) + uf(v[4 + pr][0])) + (uf(v[8 + pr][0]) + uf(v[12 + pr][0]));
    o[2 * pr + 1] = (uf(v[pr][2]) + uf(v[4 + pr][2])) + (uf(v[8 + pr][2]) + uf(v[12 + pr][2]));
  }
}

typedef __attribute__((address_space(3))) unsigned lds_u32;
__device__ __forceinline__ void lds_set(unsigned char* lds, int word) {
  asm volatile("s_waitcnt lgkmcnt(0)" ::: "memory");
  __hip_atomic_store((lds_u32*)(lds + L_W) + word, 1u, __ATOMIC_RELAXED, __HIP_MEMORY_SCOPE_WORKGROUP);
}
__device__ __forceinline__ void lds_wait(unsigned char* lds, int word, unsigned want) {
#pragma nounroll
  for (unsigned spin = 0; spin < (SPIN_CAP << 4); ++spin) {
    const unsigned v = __hip_atomic_load((lds_u32*)(lds + L_W) + word, __ATOMIC_RELAXED, __HIP_MEMORY_SCOPE_WORKGROUP);
    if (__builtin_amdgcn_readfirstlane(v) >= want) break;
    __builtin_amdgcn_s_sleep(LSLEEP);
  }
  asm volatile("" ::: "memory");
}
__device__ __forceinline__ unsigned lds_peek(unsigned char* lds, int word) {
  return __builtin_amdgcn_readfirstlane(__hip_atomic_load((lds_u32*)(lds + L_W) + word, __ATOMIC_RELAXED, __HIP_MEMORY_SCOPE_WORKGROUP));
}
__device__ __forceinline__ void lds_spin(unsigned char* lds, int word, unsigned want) {
#pragma nounroll
  for (unsigned spin = 0; spin < (SPIN_CAP << 5); ++spin) { if (lds_peek(lds, word) >= want) break; }
  asm volatile("" ::: "memory");
}
enum { G_KISSUED, G_VISSUED, G_MAXES, G_PREADY, G_ARRIVE, G_XN0, G_XN1, G_U };
typedef __attribute__((address_space(3))) v4u lds_v4u;
__device__ __forceinline__ void grp_set(unsigned char* lds, int grp, int i) {
  asm volatile("s_waitcnt lgkmcnt(0)" ::: "memory");
  __hip_atomic_store((lds_u32*)(lds + L_G + grp * 16) + i, 1u, __ATOMIC_RELAXED, __HIP_MEMORY_SCOPE_WORKGROUP);
}
__device__ __forceinline__ unsigned grp_count(unsigned char* lds, int grp) {
  const v4u f = *(volatile lds_v4u*)(lds + L_G + grp * 16);
  return __builtin_amdgcn_readfirstlane((f[0] + f[1]) + (f[2] + f[3]));
}
template <int SLEEP> __device__ __forceinline__ void grp_wait(unsigned char* lds, int grp, unsigned want) {
#pragma nounroll
  for (unsigned spin = 0; spin < (SPIN_CAP << 5); ++spin) { if (grp_count(lds, grp) >= want) break; if (SLEEP) __builtin_amdgcn_s_sleep(SLEEP); }
  asm volatile("" ::: "memory");
}
constexpr int XOR2 = 0x4E, HMIR = 0x141;
__device__ __forceinline__ float wave_sum(float x) {
  x += dpp<XOR1>(x); x += dpp<XOR2>(x); x += dpp<HMIR>(x); x += dpp<ROR8>(x);
  return xrow_sum(x);
}
__device__ __forceinline__ v4u ln_row(v4u xr) {
  float xv[8];
#pragma unroll
  for (int c = 0; c < 4; ++c) { xv[2 * c] = bflo(xr[c]); xv[2 * c + 1] = bfhi(xr[c]); }
  const float mean = wave_sum(((xv[0] + xv[1]) + (xv[2] + xv[3])) + ((xv[4] + xv[5]) + (xv[6] + xv[7]))) * (1.f / DM);
  float q = 0.f;
#pragma unroll
  for (int c = 0; c < 8; ++c) { const float d = xv[c] - mean; q = fmaf(d, d, q); }
  const float var = wave_sum(q) * (1.f / DM);
  const float mb = rbf(mean);
  const float den = rbf(sqrtf(rbf(rbf(var) + 1e-5f)));
  const float r = 1.f / den;
  v4u o;
#pragma unroll
  for (int k = 0; k < 4; ++k) {
    const unsigned n2 = pk2(xv[2 * k] - mb, xv[2 * k + 1] - mb);
    const float n0 = bflo(n2), n1 = bfhi(n2);
    const float t0 = n0 * r, t1 = n1 * r;
    o[k] = pk2(fmaf(fmaf(-t0, den, n0), r, t0), fmaf(fmaf(-t1, den, n1), r, t1));
  }
  return o;
}
__device__ __forceinline__ void cnt_add(unsigned char* ws, unsigned off) {
  __hip_atomic_fetch_add((unsigned*)(ws + off), 1u, __ATOMIC_RELAXED, __HIP_MEMORY_SCOPE_AGENT);
}
#define CNT_PEEK(r) "v_mov_b32 " r ", 0\n\tbuffer_atomic_add " r ", %2, %3, 0 offen sc0"
__device__ __forceinline__ void cnt_wait(v4u srd, unsigned off, int lane, int n, unsigned want, unsigned& r0, unsigned& r1, unsigned slack = 0u) {
  want -= slack;
  const bool idle = lane >= n;
  off = idle ? OOB : off;
  r1 = 0u;
  asm volatile(CNT_PEEK("%0") : "=&v"(r0), "+v"(r1) : "v"(off), "s"(srd) : "memory");
  __builtin_amdgcn_s_sleep(2);
#pragma nounroll
  for (unsigned spin = 0; spin < HINT_CAP; ++spin) {
    asm volatile(CNT_PEEK("%1") "\n\ts_waitcnt vmcnt(1)" : "+v"(r0), "+v"(r1) : "v"(off), "s"(srd) : "memory");
    if (__all(idle || r0 - want <= slack)) break;
    asm volatile(CNT_PEEK("%0") "\n\ts_waitcnt vmcnt(1)" : "+v"(r0), "+v"(r1) : "v"(off), "s"(srd) : "memory");
    if (__all(idle || r1 - want <= slack)) break;
  }
}
#define CNT_KEEP(r0, r1) asm volatile("" :: "v"(r0), "v"(r1))
__device__ __forceinline__ v4u mksrd(const void* p, unsigned bytes) {
  const u64 a = (u64)p;
  return v4u{(unsigned)a, (unsigned)(a >> 32) & 0xffffu, bytes, 0x00020000u};
}
__device__ __forceinline__ v2u get_tag(unsigned char* lds) {
  lds_wait(lds, SW_TREADY, 1u);
  const v2u t = *(const v2u*)(lds + L_T);
  return v2u{(unsigned)__builtin_amdgcn_readfirstlane(t[0]), (unsigned)__builtin_amdgcn_readfirstlane(t[1])};
}
struct Params {
  const u16* x; const u16* kc; const u16* vc; const u16* wq; const u16* wo; const u16* wg; const u16* wu; const u16* wd;
  u16* out; unsigned char* ws;
};


__global__ void __launch_bounds__(NTHR, 3) fused_decode(Params p) {
  extern __shared__ __attribute__((aligned(16))) unsigned char lds[];
  const int tid = threadIdx.x, lane = tid & 63;
  const int wave = __builtin_amdgcn_readfirstlane(tid >> 6);
  u64 pc0; asm volatile("s_getpc_b64 %0" : "=s"(pc0));
  if (wave == W_O && (blockIdx.x >> 3) < PFN) {
    unsigned pf0, pf1;
    const unsigned char* cp = (const unsigned char*)(pc0 & ~127ull) + lane * 128;
    asm volatile("global_load_dword %0, %2, off\n\tglobal_load_dword %1, %3, off\n\ts_barrier\n\ts_waitcnt vmcnt(0)" : "=&v"(pf0), "=&v"(pf1) : "v"(cp), "v"(cp + (CODE_LINES - 64) * 128) : "memory");
  } else {
    if (tid < 64) *(unsigned*)(lds + L_W + tid * 4) = 0u;
    BARRIER();
  }
  v4u xr = {0u, 0u, 0u, 0u};
  if (wave >= W_K0) xr = *(const v4u*)(p.x + (wave - W_K0) * DM + lane * 8);
  if (wave == W_C) {
    u64 E; asm volatile("s_load_dwordx2 %0, %1, 0x0\n\ts_waitcnt lgkmcnt(0)" : "=s"(E) : "s"(p.ws) : "memory");
    const u64 T64 = E + EPOCH_STEP;
    if (lane == 0) *(v2u*)(lds + L_T) = v2u{(unsigned)T64, (unsigned)(T64 >> 32)};
    lds_set(lds, SW_TREADY);
  }
  SCHED_FENCE();
  const int w = blockIdx.x, g = w & 7, j = w >> 3;
  const int n16 = lane & 15, x4 = lane >> 4;
  const int ob = 2 * x4 + (n16 >> 3), oco = n16 & 7;
  const int ab = 2 * (n16 >> 2) + ((n16 & 3) >> 1), aks = n16 & 1;
  const int k4 = j >> 3, m8 = j & 7;
  const int f_t = k4 * 8 + g;
  const bool has_f = f_t < 28;
  const int a_b = j >> 2, a_s = j & 3;
  const bool is_q = wave == W_Q, is_f = wave == W_F, is_o = wave == W_O;
  constexpr float LOG2E = 1.4426950408889634f;
  const unsigned wt_row = 32 * (n16 >> 3) + 8 * x4;

  if (wave >= W_K0) {
    {
      const int row = wave - W_K0;
      *(v4u*)(lds + L_XN + row * XN_STRIDE + lane * 16) = ln_row(xr);
      grp_set(lds, G_XN0 + (row >> 2), row & 3);
    }
    unsigned char* ws = p.ws; asm volatile("; role A" : "+s"(ws));
    const rsrc_t rs = mkrs(ws, WS_END);
    const bool is_k = wave < W_V0;
    const int a = (wave - W_K0) & 3;
    const size_t kvb = ((size_t)(a_b * HKV + g) * SK + (SK - WIN) + (8 * a + ((a_s + 2 * a) & 3)) * 32) * (DH * 2);
    const unsigned T2 = (4 + ((a_s + 2 * a + 1) & 3) - ((a_s + 2 * a) & 3)) * 8192;
    const int blk = 2 * a + (is_k ? 0 : 1);
    const unsigned off_op = lane < (is_k ? 17 : 16) ? WS_OP + (g * 32 + j) * 2560 + a * 640 + (is_k ? 0 : 384) + lane * 16 : OOB;
    float hmax = 0.f, hsum = 0.f;
    const unsigned char* osrc = lds + L_O + ((lane >> 3) & 1) * 1024 + (blk * 8 + (lane & 7)) * 16;
    float inv;
    v2u T;
    if (is_k) {
      v4u r[16];
      {
        const rsrc_t rs1 = mkrs((const unsigned char*)p.kc + kvb, T2 + 8192);
        const unsigned vo = (n16 >> 2) * 256 + x4 * 64 + (n16 & 3) * 16;
        SCHED_FENCE(); lds_wait(lds, SW_FISSUED, 1u); SCHED_FENCE();
#pragma unroll
        for (int t = 0; t < 16; ++t) {
          if (t == TWIN) { lds_spin(lds, SW_QPUB, 1u); SCHED_FENCE(); }
          if (t == TWIN2) { lds_spin(lds, SW_GO, 1u); SCHED_FENCE(); }
          r[t] = __builtin_amdgcn_raw_buffer_load_b128(rs1, vo + (t & 3) * 1024, ((t >> 2) & 1) * 4096 + (t >> 3) * T2, AUX_NT);
          SCHED_FENCE();
        }
        grp_set(lds, G_KISSUED, a);
      }
      T = get_tag(lds);
      lds_wait(lds, SW_QREADY, 1u);
      const v4u qf = *(const v4u*)(lds + L_Q + lane * 16);
      float sc[16];
#pragma unroll
      for (int t = 0; t < 16; t += 2) {
        const v4f d = mfma(r[t], qf, v4f{0.f, 0.f, 0.f, 0.f});
        const v4f e = mfma(r[t + 1], qf, v4f{0.f, 0.f, 0.f, 0.f});
        float v0 = dpp<0x00>(d[0]); v0 += dpp<0x55>(d[1]); v0 += dpp<0xAA>(d[2]); v0 += dpp<0xFF>(d[3]);
        float v1 = dpp<0x00>(e[0]); v1 += dpp<0x55>(e[1]); v1 += dpp<0xAA>(e[2]); v1 += dpp<0xFF>(e[3]);
        const unsigned u0 = pk2(v0, v1);
        const unsigned u1 = pk2(bflo(u0) * 0.08838834764831845f, bfhi(u0) * 0.08838834764831845f);
        sc[t] = bflo(u1); sc[t + 1] = bfhi(u1);
      }
      float mx = sc[0];
#pragma unroll
      for (int t = 1; t < 16; ++t) mx = fmaxf(mx, sc[t]);
      mx = xrow_max(mx);
      const int hs = n16 >> 2;
      if (lane < 16 && (lane & 3) == 0) *(float*)(lds + L_MX + (hs * 4 + a) * 4) = mx;
      grp_set(lds, G_MAXES, a);
      grp_wait<0>(lds, G_MAXES, 4u);
      { const v4f m0 = *(const v4f*)(lds + L_MX + hs * 16); mx = fmaxf(fmaxf(m0[0], m0[1]), fmaxf(m0[2], m0[3])); }
      float pr[16], ls = 0.f;
#pragma unroll
      for (int t = 0; t < 16; ++t) { pr[t] = __builtin_amdgcn_exp2f((sc[t] - mx) * LOG2E); ls += pr[t]; }
      ls = xrow_sum(ls);
      if (lane < 16 && (lane & 3) == 0) *(float*)(lds + L_LS + (hs * 4 + a) * 4) = ls;
      *(v4u*)(lds + L_P + (2 * a) * 1024 + lane * 16) = v4u{pk2(pr[0], pr[1]), pk2(pr[2], pr[3]), pk2(pr[4], pr[5]), pk2(pr[6], pr[7])};
      *(v4u*)(lds + L_P + (2 * a + 1) * 1024 + lane * 16) = v4u{pk2(pr[8], pr[9]), pk2(pr[10], pr[11]), pk2(pr[12], pr[13]), pk2(pr[14], pr[15])};
      grp_set(lds, G_PREADY, a);
      grp_wait<LSLEEP>(lds, G_PREADY, 4u);
      const v4f l0 = *(const v4f*)(lds + L_LS + a * 16), m0 = *(const v4f*)(lds + L_MX + a * 16);
      hsum = (l0[0] + l0[1]) + (l0[2] + l0[3]);
      hmax = fmaxf(fmaxf(m0[0], m0[1]), fmaxf(m0[2], m0[3]));
      inv = __builtin_amdgcn_rcpf(hsum);
      grp_wait<LSLEEP>(lds, G_ARRIVE, 3u);
    } else {
      v4u r[16];
      {
        const rsrc_t rs2 = mkrs((const unsigned char*)p.vc + kvb, T2 + 8192);
        SCHED_FENCE(); grp_wait<LSLEEP>(lds, G_KISSUED, 4u); SCHED_FENCE();
#pragma unroll
        for (int t = 0; t < 16; ++t) r[t] = __builtin_amdgcn_raw_buffer_load_b128(rs2, lane * 16 + (t & 3) * 1024, ((t >> 2) & 1) * 4096 + (t >> 3) * T2, AUX_NT);
        SCHED_FENCE();
        grp_set(lds, G_VISSUED, a);
      }
      T = get_tag(lds);
      grp_wait<LSLEEP>(lds, G_PREADY, 4u);
      const v4u p0 = *(const v4u*)(lds + L_P + (2 * a) * 1024 + lane * 16), p1 = *(const v4u*)(lds + L_P + (2 * a + 1) * 1024 + lane * 16);
      { const v4f l0 = *(const v4f*)(lds + L_LS + a * 16); inv = __builtin_amdgcn_rcpf((l0[0] + l0[1]) + (l0[2] + l0[3])); }
      asm volatile("" : "+v"(inv));
      v4f acc[8];
#pragma unroll
      for (int h = 0; h < 2; ++h) {
        v4u bt[8];
#pragma unroll
        for (int u = 0; u < 4; ++u) {
          SCHED_FENCE();
#pragma unroll
          for (int c = 0; c < 4; ++c) {
            bt[2 * c][u] = __builtin_amdgcn_perm(r[8 * h + 2 * u + 1][c], r[8 * h + 2 * u][c], 0x05040100u);
            bt[2 * c + 1][u] = __builtin_amdgcn_perm(r[8 * h + 2 * u + 1][c], r[8 * h + 2 * u][c], 0x07060302u);
          }
        }
#pragma unroll
        for (int e = 0; e < 8; ++e) acc[e] = mfma(h ? p1 : p0, bt[e], h ? acc[e] : v4f{0.f, 0.f, 0.f, 0.f});
      }
      *(v4f*)(lds + L_O + a * 2048 + lane * 16) = v4f{acc[0][0], acc[1][0], acc[2][0], acc[3][0]};
      *(v4f*)(lds + L_O + a * 2048 + 1024 + lane * 16) = v4f{acc[4][0], acc[5][0], acc[6][0], acc[7][0]};
      grp_set(lds, G_ARRIVE, a);
    }
    grp_wait<0>(lds, G_ARRIVE, 4u);
    {
      const v4f o0 = *(const v4f*)(osrc), o1 = *(const v4f*)(osrc + 2048), o2 = *(const v4f*)(osrc + 4096), o3 = *(const v4f*)(osrc + 6144);
      const v4f o = ((o0 + o1) + (o2 + o3)) * inv;
      mb_st(rs, off_op, v4u{lane == 16 ? __float_as_uint(hmax) : pk2(o[0], o[1]), T[0], lane == 16 ? __float_as_uint(hsum) : pk2(o[2], o[3]), T[1]});
      if (wave == W_K0) {
        if (lane < C2REP) cnt_add(ws, WS_C2 + (g * 32 + lane) * 128);
        lds_set(lds, SW_ATTDONE);
      }
    }
    return;
  }

  if (wave == W_C) {
    unsigned char* ws = p.ws; asm volatile("; role C" : "+s"(ws));
    const rsrc_t rs = mkrs(ws, WS_END);
    const v4u srd = mksrd(ws, WS_END);
    const v2u T = get_tag(lds);
    const unsigned co = WS_C3 + ((m8 * 8 + g) * 8) * 128;
    {
      const unsigned so = lane == 0 ? (j < C2REP ? WS_C2 + (g * 32 + j) * 128 : OOB) : (lane < 6 && k4 == 0) ? co + (lane - 1) * 128 : OOB;
      mb_st1(rs, so, T[0]);
    }
    const int fidx = g * DM + 64 * m8 + 8 * (lane & 7) + 2 * k4 + (x4 & 1);
    lds_wait(lds, SW_QPUB, 1u);
    __builtin_amdgcn_s_sleep(Q_DELAY);
    {
      const int qcb = 2 * (n16 >> 2) + (x4 >> 1);
      const unsigned qo = WS_QP + (g * 32 + qcb) * 4096 + ((a_b + qcb) & 7) * 128 + (4 * (x4 & 1) + (n16 & 3)) * 16;
      v4u v[16];
#pragma nounroll
      for (unsigned spin = 0; spin < SPIN_CAP; ++spin) {
        asm volatile("" ::: "memory");
#pragma unroll
        for (int k = 0; k < 16; ++k) v[k] = __builtin_amdgcn_raw_buffer_load_b128(rs, qo + (k & 3) * 1024, (k >> 2) * 32768, AUX_SC1);
        SCHED_FENCE(); lds_set(lds, SW_GO); SCHED_FENCE();
        if (gall(v, T)) break;
      }
      float q[8];
      sum4(v, q);
      *(v4u*)(lds + L_Q + lane * 16) = v4u{pk2(q[0], q[1]), pk2(q[2], q[3]), pk2(q[4], q[5]), pk2(q[6], q[7])};
    }
    lds_set(lds, SW_QREADY);
    const float xres = bflo(p.x[fidx]);
    {
      unsigned c0, c1;
      lds_wait(lds, SW_ATTDONE, 1u);
      __builtin_amdgcn_s_sleep(24);
      cnt_wait(srd, co, lane, 1, T[0] + 32u, c0, c1, K3);
      if (lds_peek(lds, SW_WODONE) == 0u) { lds_spin(lds, SW_WODONE, 1u); __builtin_amdgcn_s_sleep(D3); }
      const unsigned so = WS_SP + w * 4096 + lane * 16;
      v4u s[4];
#pragma nounroll
      for (unsigned spin = 0; spin < SPIN_CAP; ++spin) {
        asm volatile("" ::: "memory");
#pragma unroll
        for (int k = 0; k < 4; ++k) s[k] = mb_ld(rs, so + k * 1024);
        if (gall(s, T)) break;
      }
      CNT_KEEP(c0, c1);
      const float v0 = (uf(s[0][0]) + uf(s[1][0])) + (uf(s[2][0]) + uf(s[3][0]));
      const float v1 = (uf(s[0][2]) + uf(s[1][2])) + (uf(s[2][2]) + uf(s[3][2]));
      float t = swap16_add(v0, v1);
      t = swap32_add(t, t);
      t += dpp<ROR8>(t);
      lds_spin(lds, SW_GATE, 1u);
      const float gate = *(const float*)(lds + L_GATE + lane * 4);
      const float r = rbf(rbf(t) * gate);
      if (lane < 32 && n16 < 8) p.out[fidx] = (u16)pk2(xres + r, 0.f);
      if (w == 0 && lane == 0) __builtin_amdgcn_raw_buffer_store_b64(T, rs, WS_EPOCH, 0, AUX_SC1);
    }
    return;
  }

  unsigned char* ws = p.ws; asm volatile("; role QFO" : "+s"(ws));
  const rsrc_t rs = mkrs(ws, WS_END);
  v2u T;
  if (is_o) {
    T = get_tag(lds);
  } else {
    v4u r1[8], r2[8];
    const int kq = is_q ? k4 : (m8 & 3);
    {
      const unsigned stride = is_q ? HQ * DH * 2 : FF * 2;
      const unsigned char* b1 = (is_q ? (const unsigned char*)(p.wq + 512 * g + 64 * m8) : (const unsigned char*)((m8 >> 2 ? p.wu : p.wg) + 64 * f_t)) + (size_t)(128 * kq) * stride;
      const rsrc_t rs1 = mkrs(b1, (is_q || has_f) ? 127 * stride + 128 : 0u);
      const unsigned vo1 = wt_row * stride + oco * 16;
      SCHED_FENCE();
      if (is_f) { lds_wait(lds, SW_QISSUED, 1u); __builtin_amdgcn_s_sleep(F_DELAY); }
      SCHED_FENCE();
#pragma unroll
      for (int t = 0; t < 8; ++t) r1[t] = __builtin_amdgcn_raw_buffer_load_b128(rs1, vo1, t * stride, AUX_NT);
      SCHED_FENCE();
#pragma unroll
      for (int t = 0; t < 8; ++t) r2[t] = __builtin_amdgcn_raw_buffer_load_b128(rs1, vo1, (64 + t) * stride, AUX_NT);
      SCHED_FENCE();
      lds_set(lds, is_q ? SW_QISSUED : SW_FISSUED);
      SCHED_FENCE();
    }
    v4f acc[8];
    float o[8];
    v4u bt[8];
    if (is_q || has_f) {
      T = get_tag(lds);
      grp_wait<0>(lds, G_XN0, 4u); grp_wait<0>(lds, G_XN1, 4u);
      const unsigned char* xa = lds + L_XN + ab * XN_STRIDE + (128 * kq + 32 * aks + 8 * x4) * 2;
      const v4u a0 = *(const v4u*)xa, a1 = *(const v4u*)(xa + 128);
      tile_tr(bt, r1);
#pragma unroll
      for (int e = 0; e < 8; ++e) acc[e] = mfma(a0, bt[e], v4f{0.f, 0.f, 0.f, 0.f});
      tile_tr(bt, r2);
#pragma unroll
      for (int e = 0; e < 8; ++e) acc[e] = mfma(a1, bt[e], acc[e]);
      wtile_out(acc, lane, o);
      const unsigned off_a = (is_q ? WS_QP + (g * 32 + j) * 4096 : WS_GU + (f_t * 8 + m8) * 4096) + ((ob + m8) & 7) * 128 + oco * 16;
#pragma unroll
      for (int pr = 0; pr < 4; ++pr) mb_st(rs, off_a + pr * 1024, gran(o[2 * pr], o[2 * pr + 1], T));
      lds_set(lds, is_q ? SW_QPUB : SW_FPUB);
    }
    if (is_f) {
      if (has_f) {
        __builtin_amdgcn_s_sleep(G_DELAY);
        {
          const unsigned go = WS_GU + (f_t * 8 + (lane >> 3)) * 4096 + ((m8 + (lane >> 3)) & 7) * 128 + (lane & 7) * 16;
          v4u v[4];
#pragma nounroll
          for (unsigned spin = 0; spin < SPIN_CAP; ++spin) {
            asm volatile("" ::: "memory");
#pragma unroll
            for (int k = 0; k < 4; ++k) v[k] = mb_ld(rs, go + k * 1024);
            if (gall(v, T)) break;
            __builtin_amdgcn_s_sleep(8);
          }
          unsigned hp[4];
#pragma unroll
          for (int k = 0; k < 4; ++k) {
            float a0 = uf(v[k][0]), a1 = uf(v[k][2]);
            a0 += dpp<ROR8>(a0); a1 += dpp<ROR8>(a1);
            a0 = swap16_add(a0, a0); a1 = swap16_add(a1, a1);
            const unsigned r2_ = pk2(a0, a1);
            auto sw = __builtin_amdgcn_permlane32_swap(r2_, r2_, false, false);
            hp[k] = pk2(bflo(sw[0]) * bflo(sw[1]), bfhi(sw[0]) * bfhi(sw[1]));
          }
          const bool hi = (lane & 8) != 0;
          mb_st(rs, lane < 16 ? WS_HB + (f_t * 8 + m8) * 256 + lane * 16 : OOB, v4u{hi ? hp[2] : hp[0], T[0], hi ? hp[3] : hp[1], T[1]});
        }
        grp_wait<LSLEEP>(lds, G_VISSUED, 4u);
        {
          const rsrc_t rs3 = mkrs(p.wd + (size_t)(64 * f_t) * DM + 64 * m8, 63 * 1024 + 128);
#pragma unroll
          for (int t = 0; t < 8; ++t) r1[t] = __builtin_amdgcn_raw_buffer_load_b128(rs3, wt_row * 1024 + oco * 16, t * 1024, AUX_NT);
        }
        SCHED_FENCE();
        v4u hf;
        {
          const unsigned ho = WS_HB + (f_t * 8 + ab) * 256 + (4 * aks + x4) * 16;
          v4u h0, h1;
#pragma nounroll
          for (unsigned spin = 0; spin < SPIN_CAP; ++spin) {
            asm volatile("" ::: "memory");
            h0 = mb_ld(rs, ho); h1 = mb_ld(rs, ho + 128);
            if (__all(gacc(gacc(0u, h0, T), h1, T) == 0u)) break;
            __builtin_amdgcn_s_sleep(8);
          }
          hf = v4u{h0[0], h0[2], h1[0], h1[2]};
        }
        tile_tr(bt, r1);
#pragma unroll
        for (int e = 0; e < 8; ++e) acc[e] = mfma(hf, bt[e], v4f{0.f, 0.f, 0.f, 0.f});
        wtile_out(acc, lane, o);
        const unsigned off_dp = WS_DP + (m8 * 8 + ob) * 4096 + f_t * 128 + oco * 16;
#pragma unroll
        for (int pr = 0; pr < 4; ++pr) mb_st(rs, off_dp + pr * (64 * 4096), gran(o[2 * pr], o[2 * pr + 1], T));
        if (lane < 8) cnt_add(ws, WS_C3 + ((m8 * 8 + lane) * 8 + 4) * 128);
      } else {
        T = get_tag(lds);
      }
    grp_wait<LSLEEP>(lds, G_ARRIVE, 2u);
    float gate;
    {
      unsigned c0, c1;
      cnt_wait(mksrd(ws, WS_END), WS_C3 + ((m8 * 8 + g) * 8 + 4) * 128, lane, 1, T[0] + 28u, c0, c1);
      const unsigned dof = WS_DP + w * 4096 + lane * 16;
      const bool d3live = lane < 32;
      v4u d[4];
#pragma nounroll
      for (unsigned spin = 0; spin < SPIN_CAP; ++spin) {
        asm volatile("" ::: "memory");
#pragma unroll
        for (int k = 0; k < 3; ++k) d[k] = mb_ld(rs, dof + k * 1024);
        d[3] = mb_ld(rs, d3live ? dof + 3072 : dof);
        if (gall(d, T)) break;
      }
      CNT_KEEP(c0, c1);
      const float v0 = (uf(d[0][0]) + uf(d[1][0])) + (uf(d[2][0]) + (d3live ? uf(d[3][0]) : 0.f));
      const float v1 = (uf(d[0][2]) + uf(d[1][2])) + (uf(d[2][2]) + (d3live ? uf(d[3][2]) : 0.f));
      float t = swap16_add(v0, v1);
      t = swap32_add(t, t);
      t += dpp<ROR8>(t);
      t = rbf(t);
      gate = rbf(t / (1.f + __expf(-t)));
    }
    *(float*)(lds + L_GATE + lane * 4) = gate;
    lds_set(lds, SW_GATE);
      return;
    }
  }
  {
    const int tt = is_o;
    v4u bt[8], r3[8]; v4f acc[8];
    grp_wait<LSLEEP>(lds, G_VISSUED, 4u);
    {
      const rsrc_t rs3 = mkrs(p.wo + (size_t)((4 * g + k4) * 128 + 64 * tt) * DM + 64 * m8, 63 * 1024 + 128);
#pragma unroll
      for (int t = 0; t < 8; ++t) r3[t] = __builtin_amdgcn_raw_buffer_load_b128(rs3, wt_row * 1024 + oco * 16, t * 1024, AUX_NT);
    }
    const unsigned lo = WS_OP + (g * 32 + (lane >> 3) * 4) * 2560 + k4 * 640 + 256;
    const unsigned oo = WS_OP + (g * 32 + (lane >> 3) * 4) * 2560 + k4 * 640 + tt * 384 + (lane & 7) * 16;
    const int asrc = (ab * 8 + 4 * aks + x4) * 4;
    const unsigned off_sp = WS_SP + (m8 * 8 + ob) * 4096 + (4 * g + k4) * 128 + oco * 16;
    SCHED_FENCE();
    unsigned c0, c1;
    cnt_wait(mksrd(ws, WS_END), WS_C2 + (g * 32 + (j & (C2REP - 1))) * 128, lane, 1, T[0] + 32u, c0, c1, K2);
    if (lds_peek(lds, SW_ATTDONE) == 0u) { lds_spin(lds, SW_ATTDONE, 1u); __builtin_amdgcn_s_sleep(D2); }
    v4u v[8], l[4];
#pragma unroll
    for (int k = 0; k < 4; ++k) l[k] = __builtin_amdgcn_raw_buffer_load_b128(rs, lo, k * 2560, AUX_SC1);
#pragma unroll
    for (int k = 0; k < 8; ++k) v[k] = __builtin_amdgcn_raw_buffer_load_b128(rs, oo + (k & 1) * 128, (k >> 1) * 2560, AUX_SC1);
    SCHED_FENCE();
    tile_tr(bt, r3);
#pragma unroll
    for (int e = 0; e < 8; ++e) asm volatile("" : "+v"(bt[e]));
    SCHED_FENCE();
#pragma nounroll
    for (unsigned spin = 0; spin < SPIN_CAP; ++spin) {
      if (gall(v, T, gacc(gacc(gacc(gacc(0u, l[0], T), l[1], T), l[2], T), l[3], T))) break;
      asm volatile("" ::: "memory");
#pragma unroll
      for (int k = 0; k < 4; ++k) l[k] = __builtin_amdgcn_raw_buffer_load_b128(rs, lo, k * 2560, AUX_SC1);
#pragma unroll
      for (int k = 0; k < 8; ++k) v[k] = __builtin_amdgcn_raw_buffer_load_b128(rs, oo + (k & 1) * 128, (k >> 1) * 2560, AUX_SC1);
    }
    CNT_KEEP(c0, c1);
    const float lm = fmaxf(fmaxf(uf(l[0][0]), uf(l[1][0])), fmaxf(uf(l[2][0]), uf(l[3][0])));
    float e0 = uf(l[0][2]) * __builtin_amdgcn_exp2f((uf(l[0][0]) - lm) * LOG2E), e1 = uf(l[1][2]) * __builtin_amdgcn_exp2f((uf(l[1][0]) - lm) * LOG2E);
    float e2 = uf(l[2][2]) * __builtin_amdgcn_exp2f((uf(l[2][0]) - lm) * LOG2E), e3 = uf(l[3][2]) * __builtin_amdgcn_exp2f((uf(l[3][0]) - lm) * LOG2E);
    { const float inv = __builtin_amdgcn_rcpf((e0 + e1) + (e2 + e3)); e0 *= inv; e1 *= inv; e2 *= inv; e3 *= inv; }
    v4u af;
#pragma unroll
    for (int c = 0; c < 4; ++c) {
      const int hf = c >> 1, wd_ = (c & 1) * 2;
      const unsigned a0 = v[hf][wd_], a1 = v[2 + hf][wd_], a2 = v[4 + hf][wd_], a3 = v[6 + hf][wd_];
      af[c] = __builtin_amdgcn_ds_bpermute(asrc, pk2(fmaf(e0, bflo(a0), e1 * bflo(a1)) + fmaf(e2, bflo(a2), e3 * bflo(a3)), fmaf(e0, bfhi(a0), e1 * bfhi(a1)) + fmaf(e2, bfhi(a2), e3 * bfhi(a3))));
    }
#pragma unroll
    for (int e = 0; e < 8; ++e) acc[e] = mfma(af, bt[e], v4f{0.f, 0.f, 0.f, 0.f});
    float o[8];
    wtile_out(acc, lane, o);
    *(v4f*)(lds + L_U + tt * 1024 + lane * 16) = tt ? v4f{o[0], o[1], o[2], o[3]} : v4f{o[4], o[5], o[6], o[7]};
    grp_set(lds, G_U, tt);
    grp_wait<0>(lds, G_U, 2u);
    {
      const v4f u = *(const v4f*)(lds + L_U + (1 - tt) * 1024 + lane * 16);
      const v4f m = tt ? v4f{o[4], o[5], o[6], o[7]} : v4f{o[0], o[1], o[2], o[3]};
      mb_st(rs, off_sp + (2 * tt) * (64 * 4096), gran(m[0] + u[0], m[1] + u[1], T));
      mb_st(rs, off_sp + (2 * tt + 1) * (64 * 4096), gran(m[2] + u[2], m[3] + u[3], T));
      if (tt == 0) {
        if (lane < 8) cnt_add(ws, WS_C3 + ((m8 * 8 + lane) * 8) * 128);
        lds_set(lds, SW_WODONE);
      }
    }
  }
}

extern "C" void kernel_launch(void* const* d_in, const int* in_sizes, int n_in, void* d_out, int out_size, void* d_ws, size_t ws_size, hipStream_t stream) {
  Params p;
  p.x = (const u16*)d_in[0]; p.kc = (const u16*)d_in[1]; p.vc = (const u16*)d_in[2]; p.wq = (const u16*)d_in[3]; p.wo = (const u16*)d_in[4];
  p.wg = (const u16*)d_in[6]; p.wu = (const u16*)d_in[7]; p.wd = (const u16*)d_in[8];
  p.out = (u16*)d_out;
  p.ws = (unsigned char*)d_ws + ((ws_size / 2) & ~(size_t)0xfffff);
  (void)hipFuncSetAttribute((const void*)fused_decode, hipFuncAttributeMaxDynamicSharedMemorySize, LDS_BYTES);
  fused_decode<<<NWG, NTHR, LDS_BYTES, stream>>>(p);
}
```
